```python
import torch

def _softmax(x, axis=-1):
    return torch.softmax(x, dim=axis)

def _gelu(x):
    return torch.nn.functional.gelu(x, approximate='tanh')

def _silu(x):
    return torch.nn.functional.silu(x)

def _sigmoid(x):
    return torch.sigmoid(x)

def _layernorm(x, axis=-1, eps=1e-05):
    m = x.mean(dim=axis, keepdim=True)
    v = x.var(dim=axis, keepdim=True, unbiased=False)
    return (x - m) / torch.sqrt(v + eps)

def _scatter_add(tgt, idx, src, axis=0):
    return tgt.clone().index_add_(axis, idx, src)

def _segment_sum(src, seg, n):
    out = torch.zeros((n,) + src.shape[1:], dtype=src.dtype)
    return out.index_add_(0, seg, src)

def _pad(x, pw):
    flat = tuple((v for pair in reversed(pw) for v in pair))
    return torch.nn.functional.pad(x, flat)
kernel_name = 'adaLN_cond__mha_block_cached__mixer_token_mlp__decode'

def setup_inputs():

    _w = lambda a: (lambda t: t.to(torch.bfloat16) if t.is_floating_point() else t)(torch.as_tensor(a))

    def _A_setup():
        B, Sq, D = (1, 1, 512)
        H, Sk, Dh = (32, 16384, 128)
        return {'x': _w((lambda *s: torch.randn(*s, dtype=torch.bfloat16) * 0.05)(B, Sq, D)), 'k_cache': _w((lambda *s: torch.randn(*s, dtype=torch.bfloat16) * 1.6)(B, H, Sk, Dh)), 'v_cache': _w((lambda *s: torch.randn(*s, dtype=torch.bfloat16) * 1.6)(B, H, Sk, Dh)), 'wq': _w((lambda *s: torch.randn(*s, dtype=torch.bfloat16) * 1.6)(D, H * Dh) * D ** (-0.5)), 'wo': _w((lambda *s: torch.randn(*s, dtype=torch.bfloat16) * 1.6)(H * Dh, D) * (H * Dh) ** (-0.5))}
    _A_in = _A_setup()

    def _B_setup():
        B, S, D, F = (1, 1, 512, 1376)
        return {'x': _w((lambda *s: torch.randn(*s, dtype=torch.bfloat16) * 0.05)(B, S, D)), 'w1': _w((lambda *s: torch.randn(*s, dtype=torch.bfloat16) * 1.6)(S, F) * S ** (-0.5)), 'b1': _w((lambda *s: torch.randn(*s, dtype=torch.bfloat16) * 1.6)(F) * 0.02), 'w2': _w((lambda *s: torch.randn(*s, dtype=torch.bfloat16) * 1.6)(F, S) * F ** (-0.5)), 'b2': _w((lambda *s: torch.randn(*s, dtype=torch.bfloat16) * 1.6)(S) * 0.02)}
    _B_in = _B_setup()
    return {'A_x': _A_in['x'], 'A_k_cache': _A_in['k_cache'], 'A_v_cache': _A_in['v_cache'], 'A_wq': _A_in['wq'], 'A_wo': _A_in['wo'], 'B_x': _B_in['x'], 'B_w1': _B_in['w1'], 'B_b1': _B_in['b1'], 'B_w2': _B_in['w2'], 'B_b2': _B_in['b2'], 'c': _w((lambda *s: torch.randn(*s, dtype=torch.bfloat16) * 0.05)(1, 512) * 8.0), 'w_mod': _w((lambda *s: torch.randn(*s, dtype=torch.bfloat16) * 0.05)(512, 6 * 512) * 512 ** (-0.5) * 8.0)}

def reference(A_x, A_k_cache, A_v_cache, A_wq, A_wo, B_x, B_w1, B_b1, B_w2, B_b2, c, w_mod):

    def _A_ref(x, k_cache, v_cache, wq, wo):
        B, Sq, D = x.shape
        H, Dh = (32, 128)
        q = (lambda x, *d: x.permute(*d))((x @ wq).reshape(B, Sq, H, Dh), 0, 2, 1, 3)
        scores = torch.einsum('bhqd,bhkd->bhqk', q, k_cache) * Dh ** (-0.5)
        p = _softmax(scores, axis=-1)
        o = torch.einsum('bhqk,bhkd->bhqd', p, v_cache)
        return (lambda x, *d: x.permute(*d))(o, 0, 2, 1, 3).reshape(B, Sq, H * Dh) @ wo

    def _B_ref(x, w1, b1, w2, b2):
        B, S, D = x.shape
        h = _layernorm(x, axis=-1)
        u = torch.einsum('bsd,sf->bdf', h, w1) + b1
        u = _gelu(u)
        y = torch.einsum('bdf,fs->bds', u, w2) + b2
        return (lambda x, *d: x.permute(*d))(y, 0, 2, 1)
    x = A_x
    mod = (_silu(c) @ w_mod).reshape(c.shape[0], 6, 1, -1)
    sa, ca, ga, sb, cb, gb = (mod[:, 0].clone(), mod[:, 1].clone(), mod[:, 2].clone(), mod[:, 3].clone(), mod[:, 4].clone(), mod[:, 5].clone())
    h = _layernorm(x, axis=-1) * (1 + ca) + sa
    x = x + ga * _A_ref(h, A_k_cache, A_v_cache, A_wq, A_wo)
    h = _layernorm(x, axis=-1) * (1 + cb) + sb
    x = x + gb * _B_ref(h, B_w1, B_b1, B_w2, B_b2)
    return x
```

```cpp
#include <hip/hip_runtime.h>
#include <hip/hip_bf16.h>
#include <stdint.h>

#define MIMIC 0

typedef unsigned v4u __attribute__((ext_vector_type(4)));
typedef __bf16 bf16x2 __attribute__((ext_vector_type(2)));
typedef unsigned short us;

__device__ __forceinline__ float bf2f(us b) { return __uint_as_float((unsigned)b << 16); }
__device__ __forceinline__ float rb(float f) {
  return f;
}
__device__ __forceinline__ float lo16(unsigned u) { return __uint_as_float(u << 16); }
__device__ __forceinline__ float hi16(unsigned u) { return __uint_as_float(u & 0xffff0000u); }
__device__ __forceinline__ float dot2(unsigned k, unsigned q, float acc) {
  return __builtin_amdgcn_fdot2_f32_bf16(__builtin_bit_cast(bf16x2, k), __builtin_bit_cast(bf16x2, q), acc, false);
}
__device__ __forceinline__ float wave_sum(float v) {
  for (int o = 32; o > 0; o >>= 1) v += __shfl_xor(v, o);
  return v;
}
__device__ __forceinline__ float wave_max(float v) {
  for (int o = 32; o > 0; o >>= 1) v = fmaxf(v, __shfl_xor(v, o));
  return v;
}
__device__ float block_sum256(float v, float* red) {
  v = wave_sum(v);
  __syncthreads();
  if ((threadIdx.x & 63) == 0) red[threadIdx.x >> 6] = v;
  __syncthreads();
  return red[0] + red[1] + red[2] + red[3];
}
__device__ void ln512(float& a, float& b, float* red) {
  float s = block_sum256(a + b, red);
  float mf = s * (1.f / 512.f);
  float d0 = a - mf, d1 = b - mf;
  float v = block_sum256(d0 * d0 + d1 * d1, red) * (1.f / 512.f);
  float m = rb(mf);
  v = rb(v);
  float den = rb(sqrtf(rb(v + 1e-5f)));
  a = rb(rb(a - m) / den);
  b = rb(rb(b - m) / den);
}

__device__ void gemv512(const float* in_lds, const us* W, int ldw, int col0, float* red, float* out) {
  int t = threadIdx.x, cg = t & 7, rg = t >> 3;
  float acc[8] = {0, 0, 0, 0, 0, 0, 0, 0};
  for (int i = 0; i < 16; ++i) {
    int k = rg + 32 * i;
    v4u w = *(const v4u*)(W + (size_t)k * ldw + col0 + cg * 8);
    float x = in_lds[k];
    for (int j = 0; j < 4; ++j) { acc[2 * j] += x * lo16(w[j]); acc[2 * j + 1] += x * hi16(w[j]); }
  }
  for (int j = 0; j < 8; ++j) red[rg * 64 + cg * 8 + j] = acc[j];
  __syncthreads();
  if (t < 64) { float s = 0; for (int r = 0; r < 32; ++r) s += red[r * 64 + t]; out[col0 + t] = rb(s); }
}

__global__ void __launch_bounds__(256) k_mod(const us* c, const us* wmod, float* mod) {
  __shared__ float in[512]; __shared__ float red[2048];
  for (int i = threadIdx.x; i < 512; i += 256) { float v = bf2f(c[i]); in[i] = rb(v / (1.f + __expf(-v))); }
  __syncthreads();
  gemv512(in, wmod, 3072, blockIdx.x * 64, red, mod);
}
__global__ void __launch_bounds__(256) k_q(const us* x, const float* mod, const us* wq, float* q) {
  __shared__ float in[512]; __shared__ float red[2048];
  int t = threadIdx.x;
  float a = bf2f(x[t]), b = bf2f(x[t + 256]);
  ln512(a, b, red);
  in[t] = rb(rb(a * rb(1.f + mod[512 + t])) + mod[t]);
  in[t + 256] = rb(rb(b * rb(1.f + mod[512 + t + 256])) + mod[t + 256]);
  __syncthreads();
  gemv512(in, wq, 4096, blockIdx.x * 64, red, q);
}
__global__ void __launch_bounds__(256) k_attn(const float* q, const us* K, const us* V, float* part) {
  __shared__ float sc[2048]; __shared__ float red[4][132];
  int h = blockIdx.x >> 3, s = blockIdx.x & 7, t = threadIdx.x, lane = t & 63, w = t >> 6, r = lane >> 4, c = lane & 15;
  const us* Kp = K + ((size_t)h * 16384 + s * 2048 + w * 512) * 128;
  const us* Vp = V + ((size_t)h * 16384 + s * 2048 + w * 512) * 128;
  float qf[8]; for (int j = 0; j < 8; ++j) qf[j] = q[h * 128 + c * 8 + j];
  float m = -INFINITY;
  for (int i = 0; i < 128; ++i) {
    v4u k = *(const v4u*)(Kp + (size_t)(i * 4 + r) * 128 + c * 8);
    float d = 0;
    for (int j = 0; j < 4; ++j) { d += qf[2 * j] * lo16(k[j]); d += qf[2 * j + 1] * hi16(k[j]); }
    d += __shfl_xor(d, 1); d += __shfl_xor(d, 2); d += __shfl_xor(d, 4); d += __shfl_xor(d, 8);
    d = rb(rb(d) * 0.08838834764831845f);
    if (c == 0) sc[w * 512 + i * 4 + r] = d;
    m = fmaxf(m, d);
  }
  m = wave_max(m);
  __syncthreads();
  float l = 0, acc[8] = {0, 0, 0, 0, 0, 0, 0, 0};
  for (int i = 0; i < 128; ++i) {
    v4u v = *(const v4u*)(Vp + (size_t)(i * 4 + r) * 128 + c * 8);
    float p = __expf(sc[w * 512 + i * 4 + r] - m);
    l += p;
    for (int j = 0; j < 4; ++j) { acc[2 * j] += p * lo16(v[j]); acc[2 * j + 1] += p * hi16(v[j]); }
  }
  for (int j = 0; j < 8; ++j) { acc[j] += __shfl_xor(acc[j], 16); acc[j] += __shfl_xor(acc[j], 32); }
  l += __shfl_xor(l, 16); l += __shfl_xor(l, 32);
  float* o = part + (size_t)(blockIdx.x * 4 + w) * 132;
  if (r == 0) for (int j = 0; j < 8; ++j) o[4 + c * 8 + j] = acc[j];
  if (lane == 0) { o[0] = m; o[1] = l; }
}
__global__ void __launch_bounds__(256) k_wo(const float* part, const us* wo, float* yp) {
  __shared__ float o[128]; __shared__ float red[4][512];
  int h = blockIdx.x, t = threadIdx.x;
  const float* p = part + (size_t)h * 32 * 132;
  float M = -INFINITY; for (int s = 0; s < 32; ++s) M = fmaxf(M, p[s * 132]);
  float L = 0; for (int s = 0; s < 32; ++s) L += __expf(p[s * 132] - M) * p[s * 132 + 1];
  if (t < 128) { float a = 0; for (int s = 0; s < 32; ++s) a += __expf(p[s * 132] - M) * p[s * 132 + 4 + t]; o[t] = rb(a / L); }
  __syncthreads();
  int cg = t & 63, rg = t >> 6;
  float acc[8] = {0, 0, 0, 0, 0, 0, 0, 0};
  for (int i = 0; i < 32; ++i) {
    int k = rg * 32 + i;
    v4u w = *(const v4u*)(wo + (size_t)(h * 128 + k) * 512 + cg * 8);
    float x = o[k];
    for (int j = 0; j < 4; ++j) { acc[2 * j] += x * lo16(w[j]); acc[2 * j + 1] += x * hi16(w[j]); }
  }
  for (int j = 0; j < 8; ++j) red[rg][cg * 8 + j] = acc[j];
  __syncthreads();
  for (int i = t; i < 512; i += 256) yp[h * 512 + i] = red[0][i] + red[1][i] + red[2][i] + red[3][i];
}
__global__ void __launch_bounds__(256) k_final(const us* x, const float* mod, const float* yp, const us* w1, const us* b1, const us* w2, const us* b2, us* out) {
  __shared__ float red[8]; __shared__ float h2[512]; __shared__ float x1s[512];
  int t = threadIdx.x;
  float a = 0, b = 0;
  for (int h = 0; h < 32; ++h) { a += yp[h * 512 + t]; b += yp[h * 512 + t + 256]; }
  a = rb(bf2f(x[t]) + rb(mod[1024 + t] * rb(a)));
  b = rb(bf2f(x[t + 256]) + rb(mod[1024 + t + 256] * rb(b)));
  x1s[t] = a; x1s[t + 256] = b;
  ln512(a, b, red);
  a = rb(rb(a * rb(1.f + mod[2048 + t])) + mod[1536 + t]);
  b = rb(rb(b * rb(1.f + mod[2048 + t + 256])) + mod[1536 + t + 256]);
  ln512(a, b, red);
  h2[t] = a; h2[t + 256] = b;
  __syncthreads();
  for (int dd = 0; dd < 4; ++dd) {
    int d = blockIdx.x * 4 + dd;
    float hv = h2[d], s = 0;
    for (int f = t; f < 1376; f += 256) {
      float u = rb(rb(hv * bf2f(w1[f])) + bf2f(b1[f]));
      float g = rb(0.5f * u * (1.f + tanhf(0.7978845608028654f * (u + 0.044715f * u * u * u))));
      s += g * bf2f(w2[f]);
    }
    s = block_sum256(s, red);
    if (t == 0) {
      float y = rb(rb(s) + bf2f(b2[0]));
      float o = x1s[d] + rb(mod[2560 + d] * y);
      __bf16 ob = (__bf16)o; out[d] = __builtin_bit_cast(us, ob);
    }
  }
}

extern "C" void kernel_launch(void* const* d_in, const int* in_sizes, int n_in, void* d_out, int out_size, void* d_ws, size_t ws_size, hipStream_t stream) {
  const us* x = (const us*)d_in[0]; const us* K = (const us*)d_in[1]; const us* V = (const us*)d_in[2];
  const us* wq = (const us*)d_in[3]; const us* wo = (const us*)d_in[4];
  const us* w1 = (const us*)d_in[6]; const us* b1 = (const us*)d_in[7]; const us* w2 = (const us*)d_in[8]; const us* b2 = (const us*)d_in[9];
  const us* c = (const us*)d_in[10]; const us* wmod = (const us*)d_in[11];
  float* ws = (float*)d_ws;
  float* mod = ws; float* q = ws + 4096; float* part = ws + 8192; float* yp = part + 1024 * 132;
  k_mod<<<48, 256, 0, stream>>>(c, wmod, mod);
  k_q<<<64, 256, 0, stream>>>(x, mod, wq, q);
  k_attn<<<256, 256, 0, stream>>>(q, K, V, part);
  k_wo<<<32, 256, 0, stream>>>(part, wo, yp);
  k_final<<<128, 256, 0, stream>>>(x, mod, yp, w1, b1, w2, b2, (us*)d_out);
}
```

```cpp
#include <hip/hip_runtime.h>
#include <stdint.h>

typedef unsigned v4u __attribute__((ext_vector_type(4)));
typedef unsigned v2u __attribute__((ext_vector_type(2)));
typedef float v4f __attribute__((ext_vector_type(4)));
typedef float v2f __attribute__((ext_vector_type(2)));
typedef __bf16 bf16x2 __attribute__((ext_vector_type(2)));
typedef unsigned short us;
typedef unsigned long long u64;
#define GAS __attribute__((address_space(1)))
#define LAS __attribute__((address_space(3)))
#define RLX_AGENT __ATOMIC_RELAXED, __HIP_MEMORY_SCOPE_AGENT

#define P_NS1 10
#define P_NS2 9
#define P_HR 33
#define P_NREP2 8
#define P_NREPS 4
#define P_PACE "4"
#define P_WOAUX AUX_NT
#define DBG_STAMPS 0
constexpr int NT = 512;
constexpr int NS1 = P_NS1, NS2 = P_NS2;
constexpr int SZ_O = 4 * NS1 + 12, SZ_S = 4 * NS2, D0 = 2 * SZ_S + 6 * SZ_O;
constexpr int HR = P_HR;
constexpr int NREP2 = P_NREP2, NREPS = P_NREPS;
static_assert(D0 <= NT && NS1 >= 5 && NS2 >= 5 && NREP2 <= 64 && (NREPS == 2 || NREPS == 4 || NREPS == 8), "knobs");
constexpr unsigned SPIN_CAP = 1u << 18;

constexpr int WS_EPOCH = 0;
constexpr int WS_CNT = 4096;
constexpr int WS_RG = 12288;
constexpr int WS_MG = 16384;
constexpr int WS_QG = 32768;
constexpr int WS_SG = 65536;
constexpr int WS_MB2 = 327680;
constexpr int WS_MB1 = 393216;
constexpr int WS_END = WS_MB1 + 64 * 12288;
constexpr int WS_DBG = 2 << 20;
constexpr unsigned SENT = 0xFFFFFFFFu;
constexpr int AUX_NT = 2, AUX_SC1 = 16;

constexpr int L_RED = 0;
constexpr int L_LN = 512;
constexpr int L_SM = 1024;
constexpr int L_Q = 1056;
constexpr int L_ML = 1120;
constexpr int L_O = 1136;
constexpr int L_Y = 2160;
constexpr int L_SB = L_Y + 4096;
constexpr int L_CB = L_SB + 512;
constexpr int L_FIN = L_CB + 512;
constexpr int L_FLAG = L_FIN + 16;
constexpr int L_WO = L_FLAG + 16;
constexpr int L_TOT = L_WO + 16384;

struct Params {
  const us *x, *K, *V, *wq, *wo, *w1, *b1, *w2, *b2, *c, *wmod;
  us* out;
  unsigned char* ws;
};

__device__ __forceinline__ float bf2f(unsigned b) { return __uint_as_float(b << 16); }
__device__ __forceinline__ float lo16(unsigned u) { return __uint_as_float(u << 16); }
__device__ __forceinline__ float hi16(unsigned u) { return __uint_as_float(u & 0xffff0000u); }
__device__ __forceinline__ float rb(float f) { return (float)(__bf16)f; }
__device__ __forceinline__ unsigned bfbits(float f) { return (unsigned)__builtin_bit_cast(us, (__bf16)f); }
__device__ __forceinline__ float dot2(unsigned k, unsigned q, float acc) {
  return __builtin_amdgcn_fdot2_f32_bf16(__builtin_bit_cast(bf16x2, k), __builtin_bit_cast(bf16x2, q), acc, false);
}
template <int CTRL> __device__ __forceinline__ float dpp(float x) {
  return __builtin_bit_cast(float, __builtin_amdgcn_mov_dpp(__builtin_bit_cast(int, x), CTRL, 0xf, 0xf, true));
}
constexpr int XOR1 = 0xB1, XOR2 = 0x4E, XOR7 = 0x141, XOR8 = 0x128, BC = 0x150;
__device__ __forceinline__ float sw16_sum(float a, float b) {
  auto s = __builtin_amdgcn_permlane16_swap(__float_as_uint(a), __float_as_uint(b), false, false);
  return __uint_as_float(s[0]) + __uint_as_float(s[1]);
}
__device__ __forceinline__ float sw32_sum(float a, float b) {
  auto s = __builtin_amdgcn_permlane32_swap(__float_as_uint(a), __float_as_uint(b), false, false);
  return __uint_as_float(s[0]) + __uint_as_float(s[1]);
}
__device__ __forceinline__ float xrow_max(float x) {
  auto s = __builtin_amdgcn_permlane16_swap(__float_as_uint(x), __float_as_uint(x), false, false);
  x = fmaxf(__uint_as_float(s[0]), __uint_as_float(s[1]));
  auto t = __builtin_amdgcn_permlane32_swap(__float_as_uint(x), __float_as_uint(x), false, false);
  return fmaxf(__uint_as_float(t[0]), __uint_as_float(t[1]));
}
__device__ __forceinline__ float xrow_sum(float x) { x = sw16_sum(x, x); return sw32_sum(x, x); }
__device__ __forceinline__ float row_sum(float x) { x += dpp<XOR1>(x); x += dpp<XOR2>(x); x += dpp<XOR7>(x); x += dpp<XOR8>(x); return x; }
__device__ __forceinline__ float wave_sum(float x) { return xrow_sum(row_sum(x)); }
__device__ __forceinline__ float ex2(float x) { return __builtin_amdgcn_exp2f(x); }
__device__ __forceinline__ float rcp(float x) { return __builtin_amdgcn_rcpf(x); }
constexpr float LOG2E = 1.4426950408889634f;
#define PINM(x) asm volatile("" : "+v"(x) :: "memory")
#define SB() __builtin_amdgcn_sched_barrier(0)
__device__ __forceinline__ void wg_barrier() {
  asm volatile("s_waitcnt lgkmcnt(0)\n\ts_barrier" ::: "memory");
}
__device__ __forceinline__ unsigned nosent(float f) { unsigned u = __float_as_uint(f); return u == SENT ? 0x7FC00000u : u; }
__device__ __forceinline__ float tr16(float (&x)[16], int c) {
#define TRS(HALF, CTRL, BIT) _Pragma("unroll") for (int h = 0; h < (HALF); ++h) { const float t1 = x[h] + dpp<CTRL>(x[h]), t2 = x[h + (HALF)] + dpp<CTRL>(x[h + (HALF)]); x[h] = (c & (BIT)) ? t2 : t1; }
  TRS(8, XOR8, 8) TRS(4, XOR7, 4) TRS(2, XOR2, 2) TRS(1, XOR1, 1)
  return x[0];
}
__device__ __forceinline__ void ln8(float (&v)[8]) {
  const float sh = __builtin_bit_cast(float, __builtin_amdgcn_readfirstlane(__builtin_bit_cast(int, v[0])));
  float s = 0.f, q = 0.f;
#pragma unroll
  for (int i = 0; i < 8; ++i) { const float d = v[i] - sh; s += d; q = fmaf(d, d, q); }
  s = row_sum(s); q = row_sum(q);
  const float u = sw16_sum(s, q), z = sw32_sum(u, u);
  const float ms = __builtin_bit_cast(float, __builtin_amdgcn_readlane(__builtin_bit_cast(int, z), 0)) * (1.f / 512.f);
  const float mq = __builtin_bit_cast(float, __builtin_amdgcn_readlane(__builtin_bit_cast(int, z), 16)) * (1.f / 512.f);
  const float var = fmaxf(fmaf(-ms, ms, mq), 0.f);
  const float mb = rb(sh + ms), rden = 1.f / rb(__builtin_amdgcn_sqrtf(rb(rb(var) + 1e-5f)));
#pragma unroll
  for (int i = 0; i < 8; ++i) v[i] = rb(rb(v[i] - mb) * rden);
}
__device__ __forceinline__ unsigned f24(float f) { const unsigned u = __float_as_uint(f); return ((u & 0x7f800000u) == 0x7f800000u ? u : u + 0x80u) >> 8; }
__device__ __forceinline__ float silu_b(float v) { return rb(v / (1.f + __expf(-v))); }

#define STAMP(i) do { } while (0)

__global__ void __launch_bounds__(512, 2) fused_kernel(Params p) {
  __shared__ __attribute__((aligned(16))) float lds[L_TOT];
  LAS unsigned* ldsu = (LAS unsigned*)lds;
  const int t = threadIdx.x, lane = t & 63, w = __builtin_amdgcn_readfirstlane(t >> 6);
  const int r = lane >> 4, c = lane & 15, rg = lane >> 3, cg = lane & 7;
  const int col3 = ((lane >> 3) & 1) * 4 + ((lane >> 4) & 1) * 2 + (lane >> 5);
  const int b = blockIdx.x, xcd = b & 7, j = b >> 3;
  const int head = xcd * 4 + (j >> 3), split = j & 7;
  const bool svc = (split & 3) == 0;
  const __amdgpu_buffer_rsrc_t rws = __builtin_amdgcn_make_buffer_rsrc((void*)p.ws, 0, WS_END, 0x00020000);
  const __amdgpu_buffer_rsrc_t rmod = __builtin_amdgcn_make_buffer_rsrc((void*)p.wmod, 0, 512 * 3072 * 2, 0x00020000);
  STAMP(0);
#define LOAD_EPOCH() __hip_atomic_load((GAS u64*)(p.ws + WS_EPOCH), RLX_AGENT)
#define TAGS(e) const unsigned tag_hi = (unsigned)(((e) + 1) >> 16), tag_lo = (unsigned)((e) + 1) << 16
#define TOK(u) ((u.y == tag_hi) & (u.w == tag_hi) & ((u.x & 0xffff0000u) == tag_lo) & ((u.z & 0xffff0000u) == tag_lo))
#define TOKF(u) ((u.y == tgf_hi) & (u.w == tgf_hi) & ((u.x & 0xff000000u) == tgf_lo) & ((u.z & 0xff000000u) == tgf_lo))
#define NSENT(u) ((u.x != SENT) & (u.y != SENT) & (u.z != SENT) & (u.w != SENT))
#define MEMB() asm volatile("" ::: "memory")
#define DRAIN() asm volatile("s_waitcnt vmcnt(0)" ::: "memory")
#define F24(u) __uint_as_float((u) << 8)
#define RED8(a, out) do { \
    _Pragma("unroll") for (int h = 0; h < 4; ++h) { const float t1 = a[h] + dpp<XOR8>(a[h]), t2 = a[h + 4] + dpp<XOR8>(a[h + 4]); a[h] = (lane & 8) ? t2 : t1; } \
    const float u0_ = sw16_sum(a[0], a[2]), u1_ = sw16_sum(a[1], a[3]); out = sw32_sum(u0_, u1_); } while (0)
  if (t < 8) ldsu[L_FLAG + t] = 0u;

  if (svc) {
    const __amdgpu_buffer_rsrc_t rwq = __builtin_amdgcn_make_buffer_rsrc((void*)p.wq, 0, 512 * 4096 * 2, 0x00020000);
    const int g = head * 2 + (split >> 2), lc = g & 15, ks = g >> 4;
    v4u wa[2], wq[8];
    unsigned cv[2];
#pragma unroll
    for (int i = 0; i < 2; ++i) wa[i] = __builtin_amdgcn_raw_buffer_load_b128(rmod, (128 * ks + 16 * w + 8 * i + rg) * 6144 + lc * 128 + cg * 16, 0, AUX_NT);
#pragma unroll
    for (int i = 0; i < 2; ++i) cv[i] = *(const GAS unsigned*)(p.c + 128 * ks + 16 * w + 8 * i + (rg & 6));
    u64 ep = LOAD_EPOCH();
    const v4u xv = *(const GAS v4u*)(p.x + lane * 8);
    if (t == 0 && split == 0) __builtin_amdgcn_raw_buffer_store_b32(0u, rws, WS_CNT + head * 256, 0, AUX_SC1);
    SB();
    __builtin_amdgcn_s_barrier();
    SB();
#pragma unroll
    for (int i = 0; i < 8; ++i) wq[i] = __builtin_amdgcn_raw_buffer_load_b128(rwq, (64 * w + 8 * i + rg) * 8192 + g * 128 + cg * 16, 0, AUX_NT);
    SB();
    {
      float a[8] = {0, 0, 0, 0, 0, 0, 0, 0};
#pragma unroll
      for (int i = 0; i < 2; ++i) {
        const float sc = silu_b((rg & 1) ? hi16(cv[i]) : lo16(cv[i]));
#pragma unroll
        for (int k = 0; k < 4; ++k) { a[2 * k] = fmaf(sc, lo16(wa[i][k]), a[2 * k]); a[2 * k + 1] = fmaf(sc, hi16(wa[i][k]), a[2 * k + 1]); }
      }
      float z; RED8(a, z);
      lds[L_RED + w * 64 + 8 * cg + col3] = z;
    }
    STAMP(16);
    wg_barrier();
    PINM(ep);
    TAGS(ep);
    const unsigned tgf_hi = (unsigned)((ep + 1) >> 8), tgf_lo = (unsigned)(ep + 1) << 24;
    if (w == 0) {
      v2f s = *(const LAS v2f*)&lds[L_RED + 2 * (lane & 31)];
#pragma unroll
      for (int ww = 1; ww < 8; ++ww) s += *(const LAS v2f*)&lds[L_RED + ww * 64 + 2 * (lane & 31)];
      const v4u gr = {tgf_lo | f24(s.x), tgf_hi, tgf_lo | f24(s.y), tgf_hi};
#pragma unroll
      for (int rep = 0; rep < NREPS; rep += 2) __builtin_amdgcn_raw_buffer_store_b128(gr, rws, WS_SG + (rep + (lane >> 5)) * 32768 + (ks * 1024 + lc * 64) * 8 + (lane & 31) * 16, 0, AUX_SC1);
    }
    STAMP(17);
    float ln;
    {
      float xx[8];
#pragma unroll
      for (int i = 0; i < 4; ++i) { xx[2 * i] = lo16(xv[i]); xx[2 * i + 1] = hi16(xv[i]); }
      ln8(xx);
      *(LAS v4f*)&lds[L_Y + w * 512 + lane * 8] = v4f{xx[0], xx[1], xx[2], xx[3]};
      *(LAS v4f*)&lds[L_Y + w * 512 + lane * 8 + 4] = v4f{xx[4], xx[5], xx[6], xx[7]};
      ln = lds[L_Y + w * 512 + t];
    }
    const int so = WS_SG + (g & (NREPS - 1)) * 32768 + ((lane & 1) * 512 + (t & ~1)) * 8;
    v4u gp[4];
    for (unsigned spins = 0; spins < SPIN_CAP; ++spins) {
      MEMB();
#pragma unroll
      for (int i = 0; i < 4; ++i) gp[i] = __builtin_amdgcn_raw_buffer_load_b128(rws, so + i * 8192, 0, AUX_SC1);
      if (__all(TOKF(gp[0]) & TOKF(gp[1]) & TOKF(gp[2]) & TOKF(gp[3]))) break;
    }
    STAMP(18);
    const float v0 = rb((F24(gp[0].x) + F24(gp[1].x)) + (F24(gp[2].x) + F24(gp[3].x))), v1 = rb((F24(gp[0].z) + F24(gp[1].z)) + (F24(gp[2].z) + F24(gp[3].z)));
    const float ot = dpp<XOR1>((lane & 1) ? v0 : v1);
    const float sa = (lane & 1) ? ot : v0, ca = (lane & 1) ? v1 : ot;
    const float hv = rb(rb(ln * rb(1.f + ca)) + sa);
    float a[8] = {0, 0, 0, 0, 0, 0, 0, 0};
#pragma unroll
    for (int i = 0; i < 8; ++i) {
      const float hh = __shfl(hv, 8 * i + rg);
#pragma unroll
      for (int k = 0; k < 4; ++k) { a[2 * k] = fmaf(hh, lo16(wq[i][k]), a[2 * k]); a[2 * k + 1] = fmaf(hh, hi16(wq[i][k]), a[2 * k + 1]); }
    }
    float z; RED8(a, z);
    lds[L_O + w * 64 + 8 * cg + col3] = z;
    STAMP(19);
    wg_barrier();
    if (w == 0 && lane < 32) {
      v2f s = *(const LAS v2f*)&lds[L_O + 2 * lane];
#pragma unroll
      for (int ww = 1; ww < 8; ++ww) s += *(const LAS v2f*)&lds[L_O + ww * 64 + 2 * lane];
      const v4u gr = {tag_lo | bfbits(s.x), tag_hi, tag_lo | bfbits(s.y), tag_hi};
      __builtin_amdgcn_raw_buffer_store_b128(gr, rws, WS_QG + g * 512 + lane * 16, 0, AUX_SC1);
    }
    STAMP(20);
  }

  u64 epoch = 0;
  float wf[128];
  unsigned m1[3], m2[3], m3[3], b2v = 0;
  if (w >= 4) epoch = LOAD_EPOCH();
  if (w < 4 || (w > 4 && !svc)) {
    const __amdgpu_buffer_rsrc_t rk = __builtin_amdgcn_make_buffer_rsrc((void*)p.K, 0, 128 << 20, 0x00020000);
    const __amdgpu_buffer_rsrc_t rv = __builtin_amdgcn_make_buffer_rsrc((void*)p.V, 0, 128 << 20, 0x00020000);
    const int ns = svc ? NS2 : NS1, nsb = (split > 0) + (split > 4);
    const int t0 = nsb * SZ_S + (split - nsb) * SZ_O + (w < 4 ? w * ns : 4 * NS1 + (w - 5) * 4);
    const int hbase = head << 22, voff = lane * 16;
    int opq = 0; asm volatile("" : "+v"(opq));
    GAS unsigned* ctr = (GAS unsigned*)(p.ws + WS_CNT + head * 256) + opq;
#define ADDR(tt) (hbase + ((((tt) + head * HR) & (NT - 1)) << 13))
#define LDU(rs, i, base) __builtin_amdgcn_raw_buffer_load_b128(rs, voff + ((i) & 3) * 1024, (base) + ((i) >> 2) * 4096, AUX_NT)
#define PACE() asm volatile("s_waitcnt vmcnt(" P_PACE ")" ::: "memory")
    v4u KB[8], VB[8], B2[8], B3[8], B4[8], B5[8];
    {
      const int a0 = ADDR(t0), a1 = ADDR(t0 + 1), a2 = ADDR(t0 + 2);
#pragma unroll
      for (int i = 0; i < 8; ++i) { PACE(); KB[i] = LDU(rk, i, a0); SB(); }
#pragma unroll
      for (int i = 0; i < 8; ++i) { PACE(); VB[i] = LDU(rv, i, a0); SB(); }
#pragma unroll
      for (int i = 0; i < 8; ++i) { PACE(); B2[i] = LDU(rk, i, a1); SB(); }
#pragma unroll
      for (int i = 0; i < 8; ++i) { PACE(); B3[i] = LDU(rv, i, a1); SB(); }
#pragma unroll
      for (int i = 0; i < 8; ++i) { PACE(); B4[i] = LDU(rk, i, a2); SB(); }
#pragma unroll
      for (int i = 0; i < 8; ++i) { PACE(); B5[i] = LDU(rv, i, a2); SB(); }
    }
    STAMP(4);
    wg_barrier();
    STAMP(5);
    const v4u q = *(const LAS v4u*)&lds[L_Q + 4 * c];
    float m = -3.0e38f, l = 0.f, pp = 0.f;
    float acc[8] = {0, 0, 0, 0, 0, 0, 0, 0};
#define CONSK(B, REFILL, rs, base) do { float d_[8]; \
      _Pragma("unroll") for (int i = 0; i < 8; ++i) { \
        float s_ = dot2(B[i][0], q[0], 0.f); s_ = dot2(B[i][1], q[1], s_); s_ = dot2(B[i][2], q[2], s_); s_ = dot2(B[i][3], q[3], s_); d_[i] = s_; \
        if (REFILL) { PINM(d_[i]); B[i] = LDU(rs, i, base); SB(); } } \
      _Pragma("unroll") for (int h = 0; h < 4; ++h) { const float t1 = d_[h] + dpp<XOR8>(d_[h]), t2 = d_[h + 4] + dpp<XOR8>(d_[h + 4]); d_[h] = (c & 8) ? t2 : t1; } \
      _Pragma("unroll") for (int h = 0; h < 2; ++h) { const float t1 = d_[h] + dpp<XOR7>(d_[h]), t2 = d_[h + 2] + dpp<XOR7>(d_[h + 2]); d_[h] = (c & 4) ? t2 : t1; } \
      { const float t1 = d_[0] + dpp<XOR2>(d_[0]), t2 = d_[1] + dpp<XOR2>(d_[1]); d_[0] = (c & 2) ? t2 : t1; } \
      float s_ = d_[0] + dpp<XOR1>(d_[0]); \
      s_ = rb(rb(s_) * 0.08838834764831845f); \
      float mt = fmaxf(s_, dpp<XOR2>(s_)); mt = fmaxf(mt, dpp<XOR7>(mt)); mt = fmaxf(mt, dpp<XOR8>(mt)); \
      const float mn = fmaxf(m, mt), al = ex2((m - mn) * LOG2E); \
      pp = ex2((s_ - mn) * LOG2E); l = fmaf(l, al, pp); m = mn; \
      _Pragma("unroll") for (int k = 0; k < 8; ++k) acc[k] *= al; } while (0)
#define PB(i) ((i) == 0 ? dpp<BC + 0>(pp) : (i) == 1 ? dpp<BC + 2>(pp) : (i) == 2 ? dpp<BC + 4>(pp) : (i) == 3 ? dpp<BC + 6>(pp) : (i) == 4 ? dpp<BC + 8>(pp) : (i) == 5 ? dpp<BC + 10>(pp) : (i) == 6 ? dpp<BC + 12>(pp) : dpp<BC + 14>(pp))
#define CONSV(B, REFILL, rs, base) do { \
      _Pragma("unroll") for (int i = 0; i < 8; ++i) { const float pi = PB(i); \
        _Pragma("unroll") for (int k = 0; k < 4; ++k) { acc[2 * k] = fmaf(pi, lo16(B[i][k]), acc[2 * k]); acc[2 * k + 1] = fmaf(pi, hi16(B[i][k]), acc[2 * k + 1]); } \
        if (REFILL) { PINM(acc[0]); PINM(acc[1]); PINM(acc[2]); PINM(acc[3]); PINM(acc[4]); PINM(acc[5]); PINM(acc[6]); PINM(acc[7]); B[i] = LDU(rs, i, base); SB(); } } } while (0)
    { const int a3 = ADDR(t0 + 3); CONSK(KB, true, rk, a3); CONSV(VB, true, rv, a3); }
    CONSK(B2, false, rk, 0);
    CONSV(B3, false, rv, 0);
    CONSK(B4, false, rk, 0);
    CONSV(B5, false, rv, 0);
    SB();
    STAMP(6);
    if (w < 4) {
      unsigned tick = 0;
      for (int n = 4; n < NT; ++n) {
        const int nxt = n < ns ? t0 + n : D0 + (int)__builtin_amdgcn_readfirstlane(tick);
        if (nxt >= NT) break;
        const int a = ADDR(nxt);
        if (n + 1 >= ns && lane == 0) tick = __hip_atomic_fetch_add(ctr, 1u, RLX_AGENT);
        SB();
        CONSK(KB, true, rk, a);
        CONSV(VB, true, rv, a);
      }
      STAMP(7);
#pragma unroll
      for (int i = 0; i < 3; ++i) { const int f = min(t + 512 * i, 1375); m1[i] = p.w1[f]; m2[i] = p.b1[f]; m3[i] = p.w2[f]; }
      b2v = p.b2[0];
      SB();
      for (unsigned spins = 0; spins < SPIN_CAP * 16; ++spins) { if (((volatile LAS unsigned*)lds)[L_FLAG + w] == 1u) break; }
#pragma unroll
      for (int i = 0; i < 16; ++i) {
        const v4u u = *(const LAS v4u*)&lds[L_WO + (w * 16 + i) * 256 + lane * 4];
#pragma unroll
        for (int k = 0; k < 4; ++k) { wf[i * 8 + 2 * k] = lo16(u[k]); wf[i * 8 + 2 * k + 1] = hi16(u[k]); }
      }
#pragma unroll
      for (int i = 0; i < 128; ++i) asm volatile("" : "+v"(wf[i]));
      SB();
    }
    CONSK(KB, false, rk, 0);
    const float mw = xrow_max(m), fr = ex2((m - mw) * LOG2E);
    const float lw = wave_sum(l * fr) * 0.5f;
    pp *= fr;
#pragma unroll
    for (int k = 0; k < 8; ++k) acc[k] *= fr;
    if (lane == 0) { lds[L_ML + 2 * w] = mw; lds[L_ML + 2 * w + 1] = lw; }
    SB();
    CONSV(VB, false, rv, 0);
    STAMP(8);
    {
      const float u0 = sw16_sum(acc[0], acc[1]), u1 = sw16_sum(acc[2], acc[3]), u2 = sw16_sum(acc[4], acc[5]), u3 = sw16_sum(acc[6], acc[7]);
      lds[L_O + w * 128 + lane] = sw32_sum(u0, u1);
      lds[L_O + w * 128 + 64 + lane] = sw32_sum(u2, u3);
    }
  } else {
    if (w == 4) {
      PINM(epoch);
      TAGS(epoch);
      v4u qg;
      for (unsigned spins = 0; spins < SPIN_CAP; ++spins) {
        MEMB();
        qg = __builtin_amdgcn_raw_buffer_load_b128(rws, WS_QG + head * 1024 + lane * 16, 0, AUX_SC1);
        if (__all(TOK(qg))) break;
        __builtin_amdgcn_s_sleep(4);
      }
      ldsu[L_Q + lane] = (qg.x & 0xffffu) | (qg.z << 16);
    }
    STAMP(4);
    wg_barrier();
    STAMP(5);
    lds[L_O + w * 128 + lane] = 0.f; lds[L_O + w * 128 + 64 + lane] = 0.f;
    if (lane == 0) { lds[L_ML + 2 * w] = -3.0e38f; lds[L_ML + 2 * w + 1] = 0.f; }
  }
  if (w >= 4) {
    const __amdgpu_buffer_rsrc_t rwo = __builtin_amdgcn_make_buffer_rsrc((void*)p.wo, 0, 4096 * 512 * 2, 0x00020000);
    const int v = w - 4;
    PINM(epoch);
    TAGS(epoch);
    if (w == 4 && b < 64) {
      const v4u sent = {SENT, SENT, SENT, SENT};
#pragma unroll
      for (int i = 0; i < 12; ++i) __builtin_amdgcn_raw_buffer_store_b128(sent, rws, WS_MB1 + b * 12288 + i * 1024 + lane * 16, 0, AUX_SC1);
      if (b < NREP2) __builtin_amdgcn_raw_buffer_store_b128(sent, rws, WS_MB2 + b * 1024 + lane * 16, 0, AUX_SC1);
      DRAIN();
      const v2u gr = {tag_lo, tag_hi};
      if (lane == 0) __builtin_amdgcn_raw_buffer_store_b64(gr, rws, WS_RG + b * 8, 0, AUX_SC1);
    }
    if (split == 2) {
      float a[8] = {0, 0, 0, 0, 0, 0, 0, 0};
      for (int i0 = 0; i0 < 16; i0 += 8) {
        v4u u[8]; unsigned cc[8];
#pragma unroll
        for (int i = 0; i < 8; ++i) {
          const int row = 128 * v + 8 * (i0 + i) + rg;
          u[i] = __builtin_amdgcn_raw_buffer_load_b128(rmod, row * 6144 + 2048 + head * 128 + cg * 16, 0, AUX_NT); cc[i] = p.c[row];
        }
#pragma unroll
        for (int i = 0; i < 8; ++i) {
          const float sc = silu_b(bf2f(cc[i]));
#pragma unroll
          for (int k = 0; k < 4; ++k) { a[2 * k] = fmaf(sc, lo16(u[i][k]), a[2 * k]); a[2 * k + 1] = fmaf(sc, hi16(u[i][k]), a[2 * k + 1]); }
        }
      }
      float z; RED8(a, z);
      lds[L_RED + v * 64 + 8 * cg + col3] = z;
      asm volatile("s_waitcnt lgkmcnt(0)" ::: "memory");
      unsigned o_ = 0; if (lane == 0) o_ = __hip_atomic_fetch_add(ldsu + L_FLAG + 4, 1u, __ATOMIC_RELAXED, __HIP_MEMORY_SCOPE_WORKGROUP);
      if (__builtin_amdgcn_readfirstlane(o_) == 3u && lane < 32) {
        const v2f s = (*(const LAS v2f*)&lds[L_RED + 2 * lane] + *(const LAS v2f*)&lds[L_RED + 64 + 2 * lane]) + (*(const LAS v2f*)&lds[L_RED + 128 + 2 * lane] + *(const LAS v2f*)&lds[L_RED + 192 + 2 * lane]);
        const v4u gr = {tag_lo | bfbits(s.x), tag_hi, tag_lo | bfbits(s.y), tag_hi};
        __builtin_amdgcn_raw_buffer_store_b128(gr, rws, WS_MG + head * 512 + lane * 16, 0, AUX_SC1);
      }
    }
#pragma unroll
    for (int i0 = 0; i0 < 16; i0 += 8) {
      v4u u[8];
#pragma unroll
      for (int i = 0; i < 8; ++i) u[i] = __builtin_amdgcn_raw_buffer_load_b128(rwo, lane * 16 + (i & 3) * 1024, (head * 128 + v * 16 + i0 + (i & 4)) * 1024, P_WOAUX);
#pragma unroll
      for (int i = 0; i < 8; ++i) *(LAS v4u*)&lds[L_WO + (v * 16 + i0 + i) * 256 + lane * 4] = u[i];
      SB();
    }
    asm volatile("s_waitcnt lgkmcnt(0)" ::: "memory");
    if (lane == 0) ((volatile LAS unsigned*)lds)[L_FLAG + v] = 1u;
#pragma unroll
    for (int i0 = 0; i0 < 16; i0 += 8) {
      v4u u[8];
#pragma unroll
      for (int i = 0; i < 8; ++i) u[i] = __builtin_amdgcn_raw_buffer_load_b128(rwo, lane * 16 + (i & 3) * 1024, (head * 128 + w * 16 + i0 + (i & 4)) * 1024, P_WOAUX);
#pragma unroll
      for (int i = 0; i < 8; ++i)
#pragma unroll
        for (int k = 0; k < 4; ++k) { wf[(i0 + i) * 8 + 2 * k] = lo16(u[i][k]); wf[(i0 + i) * 8 + 2 * k + 1] = hi16(u[i][k]); }
#pragma unroll
      for (int i = 0; i < 64; ++i) asm volatile("" : "+v"(wf[i0 * 8 + i]));
      SB();
    }
#pragma unroll
    for (int i = 0; i < 3; ++i) { const int f = min(t + 512 * i, 1375); m1[i] = p.w1[f]; m2[i] = p.b1[f]; m3[i] = p.w2[f]; }
    STAMP(9);
    if (w == 7) {
      const unsigned xr = p.x[8 * (b & 63) + cg];
      for (unsigned spins = 0; spins < SPIN_CAP; ++spins) {
        v4u sg[4], cb_[4], gb_; v2u ga_, rg_;
        MEMB();
#pragma unroll
        for (int i = 0; i < 4; ++i) {
          sg[i] = __builtin_amdgcn_raw_buffer_load_b128(rws, WS_MG + (512 + 8 * lane + 2 * i) * 8, 0, AUX_SC1);
          cb_[i] = __builtin_amdgcn_raw_buffer_load_b128(rws, WS_MG + (1024 + 8 * lane + 2 * i) * 8, 0, AUX_SC1);
        }
        ga_ = __builtin_amdgcn_raw_buffer_load_b64(rws, WS_MG + (8 * (b & 63) + cg) * 8, 0, AUX_SC1);
        gb_ = __builtin_amdgcn_raw_buffer_load_b128(rws, WS_MG + (1536 + 2 * b) * 8, 0, AUX_SC1);
        rg_ = __builtin_amdgcn_raw_buffer_load_b64(rws, WS_RG + lane * 8, 0, AUX_SC1);
        bool ok = (ga_.y == tag_hi) & ((ga_.x & 0xffff0000u) == tag_lo) & (rg_.y == tag_hi) & (rg_.x == tag_lo) & TOK(gb_);
#pragma unroll
        for (int i = 0; i < 4; ++i) ok = ok & TOK(sg[i]) & TOK(cb_[i]);
        *(LAS v4f*)&lds[L_SB + 8 * lane] = v4f{lo16(sg[0].x), lo16(sg[0].z), lo16(sg[1].x), lo16(sg[1].z)};
        *(LAS v4f*)&lds[L_SB + 8 * lane + 4] = v4f{lo16(sg[2].x), lo16(sg[2].z), lo16(sg[3].x), lo16(sg[3].z)};
        *(LAS v4f*)&lds[L_CB + 8 * lane] = v4f{rb(1.f + lo16(cb_[0].x)), rb(1.f + lo16(cb_[0].z)), rb(1.f + lo16(cb_[1].x)), rb(1.f + lo16(cb_[1].z))};
        *(LAS v4f*)&lds[L_CB + 8 * lane + 4] = v4f{rb(1.f + lo16(cb_[2].x)), rb(1.f + lo16(cb_[2].z)), rb(1.f + lo16(cb_[3].x)), rb(1.f + lo16(cb_[3].z))};
        if (lane < 8) { lds[L_SM + lane] = lo16(ga_.x); lds[L_SM + 8 + lane] = bf2f(xr); }
        if (lane == 0) *(LAS v2f*)&lds[L_SM + 16] = v2f{lo16(gb_.x), lo16(gb_.z)};
        if (__all(ok)) break;
        __builtin_amdgcn_s_sleep(127);
      }
    }
    STAMP(10);
  }
  float f1[3], f2[3], f3[3];
#pragma unroll
  for (int i = 0; i < 3; ++i) { f1[i] = bf2f(m1[i]); f2[i] = bf2f(m2[i]); f3[i] = (t + 512 * i < 1376) ? bf2f(m3[i]) : 0.f; }
#pragma unroll
  for (int i = 0; i < 3; ++i) { asm volatile("" : "+v"(f1[i]), "+v"(f2[i]), "+v"(f3[i])); }
  wg_barrier();
  float Mw, Lw;
  {
    float y[8];
    const v2f mlc = *(const LAS v2f*)&lds[L_ML + 2 * cg];
    const float ma = lds[L_ML + 2 * r], mb = lds[L_ML + 8 + 2 * r];
    const int k_ = 16 * w + c, oi = ((k_ >> 2) & 1) * 64 + (k_ & 3) * 16 + (k_ >> 3);
    const float oa = lds[L_O + r * 128 + oi], ob = lds[L_O + 512 + r * 128 + oi];
    Mw = fmaxf(mlc.x, dpp<XOR1>(mlc.x)); Mw = fmaxf(Mw, dpp<XOR2>(Mw)); Mw = fmaxf(Mw, dpp<XOR7>(Mw));
    const float os = xrow_sum(fmaf(oa, ex2((ma - Mw) * LOG2E), ob * ex2((mb - Mw) * LOG2E)));
    Lw = ex2((mlc.x - Mw) * LOG2E) * mlc.y; Lw += dpp<XOR1>(Lw); Lw += dpp<XOR2>(Lw); Lw += dpp<XOR7>(Lw);
#pragma unroll
    for (int k = 0; k < 8; ++k) y[k] = 0.f;
#pragma unroll
    for (int i = 0; i < 16; ++i) {
      const float sk = __builtin_bit_cast(float, __builtin_amdgcn_readlane(__builtin_bit_cast(int, os), i));
#pragma unroll
      for (int k = 0; k < 8; ++k) y[k] = fmaf(sk, wf[i * 8 + k], y[k]);
    }
    *(LAS v4f*)&lds[L_Y + w * 512 + 8 * lane] = v4f{y[0], y[1], y[2], y[3]};
    *(LAS v4f*)&lds[L_Y + w * 512 + 8 * lane + 4] = v4f{y[4], y[5], y[6], y[7]};
  }
  wg_barrier();
  STAMP(11);
  if (w < 3) {
    const int rd = t / 3, part = t - 3 * rd;
    v4f s = *(const LAS v4f*)&lds[L_Y + 8 * rd + 4 * (part & 1)];
#pragma unroll
    for (int v = 1; v < 8; ++v) s += *(const LAS v4f*)&lds[L_Y + v * 512 + 8 * rd + 4 * (part & 1)];
    if (part == 2) s = v4f{Mw, Lw, 0.f, 0.f};
    const v4u msg = {nosent(s.x), nosent(s.y), nosent(s.z), nosent(s.w)};
    __builtin_amdgcn_raw_buffer_store_b128(msg, rws, WS_MB1 + rd * 12288 + (head * 8 + split) * 48 + part * 16, 0, AUX_SC1);
  }
  if (w >= 4 && b < 64) {
    v4u ms[3];
    for (unsigned spins = 0; spins < SPIN_CAP; ++spins) {
      MEMB();
#pragma unroll
      for (int i = 0; i < 3; ++i) ms[i] = __builtin_amdgcn_raw_buffer_load_b128(rws, WS_MB1 + b * 12288 + (t - 256) * 48 + i * 16, 0, AUX_SC1);
      if (__all(NSENT(ms[0]) & NSENT(ms[1]) & NSENT(ms[2]))) break;
    }
    STAMP(12);
    const float mm = __uint_as_float(ms[2].x);
    float M = fmaxf(mm, dpp<XOR1>(mm)); M = fmaxf(M, dpp<XOR2>(M)); M = fmaxf(M, dpp<XOR7>(M));
    const float e = ex2((mm - M) * LOG2E);
    float L = e * __uint_as_float(ms[2].y); L += dpp<XOR1>(L); L += dpp<XOR2>(L); L += dpp<XOR7>(L);
    const float sc = e * rcp(L);
    float a[8];
#pragma unroll
    for (int k = 0; k < 4; ++k) { a[k] = sc * __uint_as_float(ms[0][k]); a[4 + k] = sc * __uint_as_float(ms[1][k]); }
    float z; RED8(a, z);
    z += dpp<XOR1>(z); z += dpp<XOR2>(z); z += dpp<XOR7>(z);
    if (cg == 0) lds[L_RED + (w - 4) * 8 + col3] = z;
    asm volatile("s_waitcnt lgkmcnt(0)" ::: "memory");
    unsigned o_ = 0; if (lane == 0) o_ = __hip_atomic_fetch_add(ldsu + L_FLAG + 5, 1u, __ATOMIC_RELAXED, __HIP_MEMORY_SCOPE_WORKGROUP);
    if (__builtin_amdgcn_readfirstlane(o_) == 3u) {
      const float at = (lds[L_RED + cg] + lds[L_RED + 8 + cg]) + (lds[L_RED + 16 + cg] + lds[L_RED + 24 + cg]);
      const unsigned xb = bfbits(lds[L_SM + 8 + cg] + rb(lds[L_SM + cg] * rb(at)));
      unsigned pk = xb | ((unsigned)__builtin_amdgcn_mov_dpp((int)xb, XOR1, 0xf, 0xf, true) << 16);
      if (pk == SENT) pk = 0x7FC07FC0u;
      const v4u msg = {(unsigned)__builtin_amdgcn_readlane((int)pk, 0), (unsigned)__builtin_amdgcn_readlane((int)pk, 2), (unsigned)__builtin_amdgcn_readlane((int)pk, 4), (unsigned)__builtin_amdgcn_readlane((int)pk, 6)};
      if (lane < NREP2) __builtin_amdgcn_raw_buffer_store_b128(msg, rws, WS_MB2 + lane * 1024 + b * 16, 0, AUX_SC1);
    }
    STAMP(13);
  }
  if (w == 7) {
    float sb[8], cb[8];
    { const v4f a0 = *(const LAS v4f*)&lds[L_SB + 8 * lane], a1 = *(const LAS v4f*)&lds[L_SB + 8 * lane + 4], c0 = *(const LAS v4f*)&lds[L_CB + 8 * lane], c1 = *(const LAS v4f*)&lds[L_CB + 8 * lane + 4];
#pragma unroll
      for (int i = 0; i < 4; ++i) { sb[i] = a0[i]; sb[4 + i] = a1[i]; cb[i] = c0[i]; cb[4 + i] = c1[i]; } }
    v4u xs;
    for (unsigned spins = 0; spins < SPIN_CAP; ++spins) {
      MEMB();
      xs = __builtin_amdgcn_raw_buffer_load_b128(rws, WS_MB2 + (b % NREP2) * 1024 + lane * 16, 0, AUX_SC1);
      if (__all(NSENT(xs))) break;
    }
    STAMP(14);
    float v[8];
#pragma unroll
    for (int i = 0; i < 4; ++i) { v[2 * i] = lo16(xs[i]); v[2 * i + 1] = hi16(xs[i]); }
    ln8(v);
#pragma unroll
    for (int i = 0; i < 8; ++i) v[i] = rb(rb(v[i] * cb[i]) + sb[i]);
    ln8(v);
    const int e_ = b & 3;
    const unsigned xw = e_ == 0 ? xs.x : e_ == 1 ? xs.y : e_ == 2 ? xs.z : xs.w;
    const float ha = e_ == 0 ? v[0] : e_ == 1 ? v[2] : e_ == 2 ? v[4] : v[6], hb = e_ == 0 ? v[1] : e_ == 1 ? v[3] : e_ == 2 ? v[5] : v[7];
    if (lane == (b >> 2)) { *(LAS v2f*)&lds[L_SM + 18] = v2f{lo16(xw), hi16(xw)}; *(LAS v2f*)&lds[L_SM + 20] = v2f{ha, hb}; }
    if (b == 0 && lane == 0) __hip_atomic_store((GAS u64*)(p.ws + WS_EPOCH), epoch + 1, RLX_AGENT);
  }
  wg_barrier();
  STAMP(15);
  {
    const v2f h2 = *(const LAS v2f*)&lds[L_SM + 20];
    float s0 = 0.f, s1 = 0.f;
#pragma unroll
    for (int i = 0; i < 3; ++i) {
#pragma unroll
      for (int d = 0; d < 2; ++d) {
        const float u = rb(rb(h2[d] * f1[i]) + f2[i]);
        const float z = u * fmaf(0.044715f * u, u, 1.f) * (-2.f * 0.7978845608028654f * LOG2E);
        const float gl = rb(u * rcp(1.f + ex2(z)));
        if (d == 0) s0 = fmaf(gl, f3[i], s0); else s1 = fmaf(gl, f3[i], s1);
      }
    }
    s0 = row_sum(s0); s1 = row_sum(s1);
    const float u = sw16_sum(s0, s1), z = sw32_sum(u, u);
    if (lane == 0 || lane == 16) lds[L_FIN + w * 2 + (lane >> 4)] = z;
  }
  wg_barrier();
  if (t < 2) {
    float s = 0.f;
#pragma unroll
    for (int v = 0; v < 8; ++v) s += lds[L_FIN + v * 2 + t];
    const float yv = rb(rb(s) + bf2f(b2v));
    const float o = lds[L_SM + 18 + t] + rb(lds[L_SM + 16 + t] * yv);
    p.out[2 * b + t] = (us)bfbits(o);
  }
  STAMP(21);
}

extern "C" void kernel_launch(void* const* d_in, const int* in_sizes, int n_in, void* d_out, int out_size, void* d_ws, size_t ws_size, hipStream_t stream) {
  Params p;
  p.x = (const us*)d_in[0]; p.K = (const us*)d_in[1]; p.V = (const us*)d_in[2]; p.wq = (const us*)d_in[3]; p.wo = (const us*)d_in[4];
  p.w1 = (const us*)d_in[6]; p.b1 = (const us*)d_in[7]; p.w2 = (const us*)d_in[8]; p.b2 = (const us*)d_in[9];
  p.c = (const us*)d_in[10]; p.wmod = (const us*)d_in[11];
  p.out = (us*)d_out; p.ws = (unsigned char*)d_ws;
  fused_kernel<<<256, 512, 0, stream>>>(p);
}
```

```cpp
#include <hip/hip_runtime.h>
#include <stdint.h>

typedef unsigned v4u __attribute__((ext_vector_type(4)));
typedef unsigned v2u __attribute__((ext_vector_type(2)));
typedef float v4f __attribute__((ext_vector_type(4)));
typedef float v2f __attribute__((ext_vector_type(2)));
typedef __bf16 bf16x2 __attribute__((ext_vector_type(2)));
typedef unsigned short us;
typedef unsigned long long u64;
#define GAS __attribute__((address_space(1)))
#define LAS __attribute__((address_space(3)))
#define RLX_AGENT __ATOMIC_RELAXED, __HIP_MEMORY_SCOPE_AGENT

#define P_NS1 10
#define P_NS2 9
#define P_HR 33
#define P_NREP2 8
#define P_NREPS 4
#define P_PACE "4"
#define P_WOAUX 0
#define P_KAUX 2
#define P_VAUX 2
#define P_WAUX 0
#define DBG_STAMPS 0
constexpr int NT = 512;
constexpr int NS1 = P_NS1, NS2 = P_NS2;
constexpr int SZ_O = 4 * NS1 + 12, SZ_S = 4 * NS2, D0 = 2 * SZ_S + 6 * SZ_O;
constexpr int HR = P_HR;
constexpr int NREP2 = P_NREP2, NREPS = P_NREPS;
static_assert(D0 <= NT && NS1 >= 5 && NS2 >= 5 && NREP2 <= 64 && (NREPS == 2 || NREPS == 4 || NREPS == 8), "knobs");
constexpr unsigned SPIN_CAP = 1u << 18;

constexpr int WS_EPOCH = 0;
constexpr int WS_CNT = 4096;
constexpr int WS_RG = 12288;
constexpr int WS_MG = 16384;
constexpr int WS_QG = 32768;
constexpr int WS_SG = 65536;
constexpr int WS_MB2 = 327680;
constexpr int WS_MB1 = 393216;
constexpr int WS_END = WS_MB1 + 64 * 12288;
constexpr int WS_DBG = 2 << 20;
constexpr unsigned SENT = 0xFFFFFFFFu;
constexpr int AUX_NT = 2, AUX_SC1 = 16;

constexpr int L_RED = 0;
constexpr int L_LN = 512;
constexpr int L_SM = 1024;
constexpr int L_Q = 1056;
constexpr int L_ML = 1120;
constexpr int L_O = 1136;
constexpr int L_Y = 2160;
constexpr int L_SB = L_Y + 4096;
constexpr int L_CB = L_SB + 512;
constexpr int L_FIN = L_CB + 512;
constexpr int L_FLAG = L_FIN + 16;
constexpr int L_WO = L_FLAG + 16;
constexpr int L_TOT = L_WO + 16384;

struct Params {
  const us *x, *K, *V, *wq, *wo, *w1, *b1, *w2, *b2, *c, *wmod;
  us* out;
  unsigned char* ws;
};

__device__ __forceinline__ float bf2f(unsigned b) { return __uint_as_float(b << 16); }
__device__ __forceinline__ float lo16(unsigned u) { return __uint_as_float(u << 16); }
__device__ __forceinline__ float hi16(unsigned u) { return __uint_as_float(u & 0xffff0000u); }
__device__ __forceinline__ float rb(float f) { return (float)(__bf16)f; }
__device__ __forceinline__ unsigned bfbits(float f) { return (unsigned)__builtin_bit_cast(us, (__bf16)f); }
__device__ __forceinline__ float dot2(unsigned k, unsigned q, float acc) {
  return __builtin_amdgcn_fdot2_f32_bf16(__builtin_bit_cast(bf16x2, k), __builtin_bit_cast(bf16x2, q), acc, false);
}
template <int CTRL> __device__ __forceinline__ float dpp(float x) {
  return __builtin_bit_cast(float, __builtin_amdgcn_mov_dpp(__builtin_bit_cast(int, x), CTRL, 0xf, 0xf, true));
}
constexpr int XOR1 = 0xB1, XOR2 = 0x4E, XOR7 = 0x141, XOR8 = 0x128, BC = 0x150;
__device__ __forceinline__ float sw16_sum(float a, float b) {
  auto s = __builtin_amdgcn_permlane16_swap(__float_as_uint(a), __float_as_uint(b), false, false);
  return __uint_as_float(s[0]) + __uint_as_float(s[1]);
}
__device__ __forceinline__ float sw32_sum(float a, float b) {
  auto s = __builtin_amdgcn_permlane32_swap(__float_as_uint(a), __float_as_uint(b), false, false);
  return __uint_as_float(s[0]) + __uint_as_float(s[1]);
}
__device__ __forceinline__ float xrow_max(float x) {
  auto s = __builtin_amdgcn_permlane16_swap(__float_as_uint(x), __float_as_uint(x), false, false);
  x = fmaxf(__uint_as_float(s[0]), __uint_as_float(s[1]));
  auto t = __builtin_amdgcn_permlane32_swap(__float_as_uint(x), __float_as_uint(x), false, false);
  return fmaxf(__uint_as_float(t[0]), __uint_as_float(t[1]));
}
__device__ __forceinline__ float xrow_sum(float x) { x = sw16_sum(x, x); return sw32_sum(x, x); }
__device__ __forceinline__ float row_sum(float x) { x += dpp<XOR1>(x); x += dpp<XOR2>(x); x += dpp<XOR7>(x); x += dpp<XOR8>(x); return x; }
__device__ __forceinline__ float wave_sum(float x) { return xrow_sum(row_sum(x)); }
__device__ __forceinline__ float ex2(float x) { return __builtin_amdgcn_exp2f(x); }
__device__ __forceinline__ float rcp(float x) { return __builtin_amdgcn_rcpf(x); }
constexpr float LOG2E = 1.4426950408889634f;
#define PINM(x) asm volatile("" : "+v"(x) :: "memory")
#define SB() __builtin_amdgcn_sched_barrier(0)
__device__ __forceinline__ void wg_barrier() {
  asm volatile("s_waitcnt lgkmcnt(0)\n\ts_barrier" ::: "memory");
}
__device__ __forceinline__ unsigned nosent(float f) { unsigned u = __float_as_uint(f); return u == SENT ? 0x7FC00000u : u; }
__device__ __forceinline__ float tr16(float (&x)[16], int c) {
#define TRS(HALF, CTRL, BIT) _Pragma("unroll") for (int h = 0; h < (HALF); ++h) { const float t1 = x[h] + dpp<CTRL>(x[h]), t2 = x[h + (HALF)] + dpp<CTRL>(x[h + (HALF)]); x[h] = (c & (BIT)) ? t2 : t1; }
  TRS(8, XOR8, 8) TRS(4, XOR7, 4) TRS(2, XOR2, 2) TRS(1, XOR1, 1)
  return x[0];
}
__device__ __forceinline__ void ln8(float (&v)[8]) {
  const float sh = __builtin_bit_cast(float, __builtin_amdgcn_readfirstlane(__builtin_bit_cast(int, v[0])));
  float s = 0.f, q = 0.f;
#pragma unroll
  for (int i = 0; i < 8; ++i) { const float d = v[i] - sh; s += d; q = fmaf(d, d, q); }
  s = row_sum(s); q = row_sum(q);
  const float u = sw16_sum(s, q), z = sw32_sum(u, u);
  const float ms = __builtin_bit_cast(float, __builtin_amdgcn_readlane(__builtin_bit_cast(int, z), 0)) * (1.f / 512.f);
  const float mq = __builtin_bit_cast(float, __builtin_amdgcn_readlane(__builtin_bit_cast(int, z), 16)) * (1.f / 512.f);
  const float var = fmaxf(fmaf(-ms, ms, mq), 0.f);
  const float mb = rb(sh + ms), rden = 1.f / rb(__builtin_amdgcn_sqrtf(rb(rb(var) + 1e-5f)));
#pragma unroll
  for (int i = 0; i < 8; ++i) v[i] = rb(rb(v[i] - mb) * rden);
}
__device__ __forceinline__ unsigned f24(float f) { const unsigned u = __float_as_uint(f); return ((u & 0x7f800000u) == 0x7f800000u ? u : u + 0x80u) >> 8; }
__device__ __forceinline__ float silu_b(float v) { return rb(v / (1.f + __expf(-v))); }

#define STAMP(i) do { } while (0)

__global__ void __launch_bounds__(512, 2) fused_kernel(Params p) {
  __shared__ __attribute__((aligned(16))) float lds[L_TOT];
  LAS unsigned* ldsu = (LAS unsigned*)lds;
  const int t = threadIdx.x, lane = t & 63, w = __builtin_amdgcn_readfirstlane(t >> 6);
  const int r = lane >> 4, c = lane & 15, rg = lane >> 3, cg = lane & 7;
  const int col3 = ((lane >> 3) & 1) * 4 + ((lane >> 4) & 1) * 2 + (lane >> 5);
  const int b = blockIdx.x, xcd = b & 7, j = b >> 3;
  const int head = xcd * 4 + (j >> 3), split = j & 7;
  const bool svc = (split & 3) == 0;
  const __amdgpu_buffer_rsrc_t rws = __builtin_amdgcn_make_buffer_rsrc((void*)p.ws, 0, WS_END, 0x00020000);
  const __amdgpu_buffer_rsrc_t rmod = __builtin_amdgcn_make_buffer_rsrc((void*)p.wmod, 0, 512 * 3072 * 2, 0x00020000);
  STAMP(0);
#define LOAD_EPOCH() __hip_atomic_load((GAS u64*)(p.ws + WS_EPOCH), RLX_AGENT)
#define TAGS(e) const unsigned tag_hi = (unsigned)(((e) + 1) >> 16), tag_lo = (unsigned)((e) + 1) << 16
#define TOK(u) ((u.y == tag_hi) & (u.w == tag_hi) & ((u.x & 0xffff0000u) == tag_lo) & ((u.z & 0xffff0000u) == tag_lo))
#define TOKF(u) ((u.y == tgf_hi) & (u.w == tgf_hi) & ((u.x & 0xff000000u) == tgf_lo) & ((u.z & 0xff000000u) == tgf_lo))
#define NSENT(u) ((u.x != SENT) & (u.y != SENT) & (u.z != SENT) & (u.w != SENT))
#define MEMB() asm volatile("" ::: "memory")
#define DRAIN() asm volatile("s_waitcnt vmcnt(0)" ::: "memory")
#define F24(u) __uint_as_float((u) << 8)
#define RED8(a, out) do { \
    _Pragma("unroll") for (int h = 0; h < 4; ++h) { const float t1 = a[h] + dpp<XOR8>(a[h]), t2 = a[h + 4] + dpp<XOR8>(a[h + 4]); a[h] = (lane & 8) ? t2 : t1; } \
    const float u0_ = sw16_sum(a[0], a[2]), u1_ = sw16_sum(a[1], a[3]); out = sw32_sum(u0_, u1_); } while (0)
  if (t < 8) ldsu[L_FLAG + t] = 0u;

  if (svc) {
    const __amdgpu_buffer_rsrc_t rwq = __builtin_amdgcn_make_buffer_rsrc((void*)p.wq, 0, 512 * 4096 * 2, 0x00020000);
    const int g = head * 2 + (split >> 2), lc = g & 15, ks = g >> 4;
    v4u wa[2], wq[8];
    unsigned cv[2];
#pragma unroll
    for (int i = 0; i < 2; ++i) wa[i] = __builtin_amdgcn_raw_buffer_load_b128(rmod, (128 * ks + 16 * w + 8 * i + rg) * 6144 + lc * 128 + cg * 16, 0, P_WAUX);
#pragma unroll
    for (int i = 0; i < 2; ++i) cv[i] = *(const GAS unsigned*)(p.c + 128 * ks + 16 * w + 8 * i + (rg & 6));
    u64 ep = LOAD_EPOCH();
    const v4u xv = *(const GAS v4u*)(p.x + lane * 8);
    if (t == 0 && split == 0) __builtin_amdgcn_raw_buffer_store_b32(0u, rws, WS_CNT + head * 256, 0, AUX_SC1);
    SB();
    __builtin_amdgcn_s_barrier();
    SB();
#pragma unroll
    for (int i = 0; i < 8; ++i) wq[i] = __builtin_amdgcn_raw_buffer_load_b128(rwq, (64 * w + 8 * i + rg) * 8192 + g * 128 + cg * 16, 0, P_WAUX);
    SB();
    {
      float a[8] = {0, 0, 0, 0, 0, 0, 0, 0};
#pragma unroll
      for (int i = 0; i < 2; ++i) {
        const float sc = silu_b((rg & 1) ? hi16(cv[i]) : lo16(cv[i]));
#pragma unroll
        for (int k = 0; k < 4; ++k) { a[2 * k] = fmaf(sc, lo16(wa[i][k]), a[2 * k]); a[2 * k + 1] = fmaf(sc, hi16(wa[i][k]), a[2 * k + 1]); }
      }
      float z; RED8(a, z);
      lds[L_RED + w * 64 + 8 * cg + col3] = z;
    }
    STAMP(16);
    wg_barrier();
    PINM(ep);
    TAGS(ep);
    const unsigned tgf_hi = (unsigned)((ep + 1) >> 8), tgf_lo = (unsigned)(ep + 1) << 24;
    if (w == 0) {
      v2f s = *(const LAS v2f*)&lds[L_RED + 2 * (lane & 31)];
#pragma unroll
      for (int ww = 1; ww < 8; ++ww) s += *(const LAS v2f*)&lds[L_RED + ww * 64 + 2 * (lane & 31)];
      const v4u gr = {tgf_lo | f24(s.x), tgf_hi, tgf_lo | f24(s.y), tgf_hi};
#pragma unroll
      for (int rep = 0; rep < NREPS; rep += 2) __builtin_amdgcn_raw_buffer_store_b128(gr, rws, WS_SG + (rep + (lane >> 5)) * 32768 + (ks * 1024 + lc * 64) * 8 + (lane & 31) * 16, 0, AUX_SC1);
    }
    STAMP(17);
    float ln;
    {
      float xx[8];
#pragma unroll
      for (int i = 0; i < 4; ++i) { xx[2 * i] = lo16(xv[i]); xx[2 * i + 1] = hi16(xv[i]); }
      ln8(xx);
      *(LAS v4f*)&lds[L_Y + w * 512 + lane * 8] = v4f{xx[0], xx[1], xx[2], xx[3]};
      *(LAS v4f*)&lds[L_Y + w * 512 + lane * 8 + 4] = v4f{xx[4], xx[5], xx[6], xx[7]};
      ln = lds[L_Y + w * 512 + t];
    }
    const int so = WS_SG + (g & (NREPS - 1)) * 32768 + ((lane & 1) * 512 + (t & ~1)) * 8;
    v4u gp[4];
    for (unsigned spins = 0; spins < SPIN_CAP; ++spins) {
      MEMB();
#pragma unroll
      for (int i = 0; i < 4; ++i) gp[i] = __builtin_amdgcn_raw_buffer_load_b128(rws, so + i * 8192, 0, AUX_SC1);
      if (__all(TOKF(gp[0]) & TOKF(gp[1]) & TOKF(gp[2]) & TOKF(gp[3]))) break;
    }
    STAMP(18);
    const float v0 = rb((F24(gp[0].x) + F24(gp[1].x)) + (F24(gp[2].x) + F24(gp[3].x))), v1 = rb((F24(gp[0].z) + F24(gp[1].z)) + (F24(gp[2].z) + F24(gp[3].z)));
    const float ot = dpp<XOR1>((lane & 1) ? v0 : v1);
    const float sa = (lane & 1) ? ot : v0, ca = (lane & 1) ? v1 : ot;
    const float hv = rb(rb(ln * rb(1.f + ca)) + sa);
    float a[8] = {0, 0, 0, 0, 0, 0, 0, 0};
#pragma unroll
    for (int i = 0; i < 8; ++i) {
      const float hh = __shfl(hv, 8 * i + rg);
#pragma unroll
      for (int k = 0; k < 4; ++k) { a[2 * k] = fmaf(hh, lo16(wq[i][k]), a[2 * k]); a[2 * k + 1] = fmaf(hh, hi16(wq[i][k]), a[2 * k + 1]); }
    }
    float z; RED8(a, z);
    lds[L_O + w * 64 + 8 * cg + col3] = z;
    STAMP(19);
    wg_barrier();
    if (w == 0 && lane < 32) {
      v2f s = *(const LAS v2f*)&lds[L_O + 2 * lane];
#pragma unroll
      for (int ww = 1; ww < 8; ++ww) s += *(const LAS v2f*)&lds[L_O + ww * 64 + 2 * lane];
      const v4u gr = {tag_lo | bfbits(s.x), tag_hi, tag_lo | bfbits(s.y), tag_hi};
      __builtin_amdgcn_raw_buffer_store_b128(gr, rws, WS_QG + g * 512 + lane * 16, 0, AUX_SC1);
    }
    STAMP(20);
  }

  u64 epoch = 0;
  float wf[128];
  unsigned m1[3], m2[3], m3[3], b2v = 0;
  if (w >= 4) epoch = LOAD_EPOCH();
  if (w < 4 || (w > 4 && !svc)) {
    const __amdgpu_buffer_rsrc_t rk = __builtin_amdgcn_make_buffer_rsrc((void*)p.K, 0, 128 << 20, 0x00020000);
    const __amdgpu_buffer_rsrc_t rv = __builtin_amdgcn_make_buffer_rsrc((void*)p.V, 0, 128 << 20, 0x00020000);
    const int ns = svc ? NS2 : NS1, nsb = (split > 0) + (split > 4);
    const int t0 = nsb * SZ_S + (split - nsb) * SZ_O + (w < 4 ? w * ns : 4 * NS1 + (w - 5) * 4);
    const int hbase = head << 22, voff = lane * 16;
    int opq = 0; asm volatile("" : "+v"(opq));
    GAS unsigned* ctr = (GAS unsigned*)(p.ws + WS_CNT + head * 256) + opq;
#define ADDR(tt) (hbase + ((((tt) + head * HR) & (NT - 1)) << 13))
#define AUX_rk P_KAUX
#define AUX_rv P_VAUX
#define LDU(rs, i, base) __builtin_amdgcn_raw_buffer_load_b128(rs, voff + ((i) & 3) * 1024, (base) + ((i) >> 2) * 4096, AUX_##rs)
#define PACE() asm volatile("s_waitcnt vmcnt(" P_PACE ")" ::: "memory")
    v4u KB[8], VB[8], B2[8], B3[8], B4[8], B5[8];
    {
      const int a0 = ADDR(t0), a1 = ADDR(t0 + 1), a2 = ADDR(t0 + 2);
#pragma unroll
      for (int i = 0; i < 8; ++i) { PACE(); KB[i] = LDU(rk, i, a0); SB(); }
#pragma unroll
      for (int i = 0; i < 8; ++i) { PACE(); VB[i] = LDU(rv, i, a0); SB(); }
#pragma unroll
      for (int i = 0; i < 8; ++i) { PACE(); B2[i] = LDU(rk, i, a1); SB(); }
#pragma unroll
      for (int i = 0; i < 8; ++i) { PACE(); B3[i] = LDU(rv, i, a1); SB(); }
#pragma unroll
      for (int i = 0; i < 8; ++i) { PACE(); B4[i] = LDU(rk, i, a2); SB(); }
#pragma unroll
      for (int i = 0; i < 8; ++i) { PACE(); B5[i] = LDU(rv, i, a2); SB(); }
    }
    STAMP(4);
    wg_barrier();
    STAMP(5);
    const v4u q = *(const LAS v4u*)&lds[L_Q + 4 * c];
    float m = -3.0e38f, l = 0.f, pp = 0.f;
    float acc[8] = {0, 0, 0, 0, 0, 0, 0, 0};
#define CONSK(B, REFILL, rs, base) do { float d_[8]; \
      _Pragma("unroll") for (int i = 0; i < 8; ++i) { \
        float s_ = dot2(B[i][0], q[0], 0.f); s_ = dot2(B[i][1], q[1], s_); s_ = dot2(B[i][2], q[2], s_); s_ = dot2(B[i][3], q[3], s_); d_[i] = s_; \
        if (REFILL) { PINM(d_[i]); B[i] = LDU(rs, i, base); SB(); } } \
      _Pragma("unroll") for (int h = 0; h < 4; ++h) { const float t1 = d_[h] + dpp<XOR8>(d_[h]), t2 = d_[h + 4] + dpp<XOR8>(d_[h + 4]); d_[h] = (c & 8) ? t2 : t1; } \
      _Pragma("unroll") for (int h = 0; h < 2; ++h) { const float t1 = d_[h] + dpp<XOR7>(d_[h]), t2 = d_[h + 2] + dpp<XOR7>(d_[h + 2]); d_[h] = (c & 4) ? t2 : t1; } \
      { const float t1 = d_[0] + dpp<XOR2>(d_[0]), t2 = d_[1] + dpp<XOR2>(d_[1]); d_[0] = (c & 2) ? t2 : t1; } \
      float s_ = d_[0] + dpp<XOR1>(d_[0]); \
      s_ = rb(rb(s_) * 0.08838834764831845f); \
      float mt = fmaxf(s_, dpp<XOR2>(s_)); mt = fmaxf(mt, dpp<XOR7>(mt)); mt = fmaxf(mt, dpp<XOR8>(mt)); \
      const float mn = fmaxf(m, mt), al = ex2((m - mn) * LOG2E); \
      pp = ex2((s_ - mn) * LOG2E); l = fmaf(l, al, pp); m = mn; \
      _Pragma("unroll") for (int k = 0; k < 8; ++k) acc[k] *= al; } while (0)
#define PB(i) ((i) == 0 ? dpp<BC + 0>(pp) : (i) == 1 ? dpp<BC + 2>(pp) : (i) == 2 ? dpp<BC + 4>(pp) : (i) == 3 ? dpp<BC + 6>(pp) : (i) == 4 ? dpp<BC + 8>(pp) : (i) == 5 ? dpp<BC + 10>(pp) : (i) == 6 ? dpp<BC + 12>(pp) : dpp<BC + 14>(pp))
#define CONSV(B, REFILL, rs, base) do { \
      _Pragma("unroll") for (int i = 0; i < 8; ++i) { const float pi = PB(i); \
        _Pragma("unroll") for (int k = 0; k < 4; ++k) { acc[2 * k] = fmaf(pi, lo16(B[i][k]), acc[2 * k]); acc[2 * k + 1] = fmaf(pi, hi16(B[i][k]), acc[2 * k + 1]); } \
        if (REFILL) { PINM(acc[0]); PINM(acc[1]); PINM(acc[2]); PINM(acc[3]); PINM(acc[4]); PINM(acc[5]); PINM(acc[6]); PINM(acc[7]); B[i] = LDU(rs, i, base); SB(); } } } while (0)
    { const int a3 = ADDR(t0 + 3); CONSK(KB, true, rk, a3); CONSV(VB, true, rv, a3); }
    CONSK(B2, false, rk, 0);
    CONSV(B3, false, rv, 0);
    CONSK(B4, false, rk, 0);
    CONSV(B5, false, rv, 0);
    SB();
    STAMP(6);
    if (w < 4) {
      unsigned tick = 0;
      for (int n = 4; n < NT; ++n) {
        const int nxt = n < ns ? t0 + n : D0 + (int)__builtin_amdgcn_readfirstlane(tick);
        if (nxt >= NT) break;
        const int a = ADDR(nxt);
        if (n + 1 >= ns && lane == 0) tick = __hip_atomic_fetch_add(ctr, 1u, RLX_AGENT);
        SB();
        CONSK(KB, true, rk, a);
        CONSV(VB, true, rv, a);
      }
      STAMP(7);
#pragma unroll
      for (int i = 0; i < 3; ++i) { const int f = min(t + 512 * i, 1375); m1[i] = p.w1[f]; m2[i] = p.b1[f]; m3[i] = p.w2[f]; }
      b2v = p.b2[0];
      SB();
      for (unsigned spins = 0; spins < SPIN_CAP * 16; ++spins) { if (((volatile LAS unsigned*)lds)[L_FLAG + w] == 1u) break; }
#pragma unroll
      for (int i = 0; i < 16; ++i) {
        const v4u u = *(const LAS v4u*)&lds[L_WO + (w * 16 + i) * 256 + lane * 4];
#pragma unroll
        for (int k = 0; k < 4; ++k) { wf[i * 8 + 2 * k] = lo16(u[k]); wf[i * 8 + 2 * k + 1] = hi16(u[k]); }
      }
#pragma unroll
      for (int i = 0; i < 128; ++i) asm volatile("" : "+v"(wf[i]));
      SB();
    }
    CONSK(KB, false, rk, 0);
    const float mw = xrow_max(m), fr = ex2((m - mw) * LOG2E);
    const float lw = wave_sum(l * fr) * 0.5f;
    pp *= fr;
#pragma unroll
    for (int k = 0; k < 8; ++k) acc[k] *= fr;
    if (lane == 0) { lds[L_ML + 2 * w] = mw; lds[L_ML + 2 * w + 1] = lw; }
    SB();
    CONSV(VB, false, rv, 0);
    STAMP(8);
    {
      const float u0 = sw16_sum(acc[0], acc[1]), u1 = sw16_sum(acc[2], acc[3]), u2 = sw16_sum(acc[4], acc[5]), u3 = sw16_sum(acc[6], acc[7]);
      lds[L_O + w * 128 + lane] = sw32_sum(u0, u1);
      lds[L_O + w * 128 + 64 + lane] = sw32_sum(u2, u3);
    }
  } else {
    if (w == 4) {
      PINM(epoch);
      TAGS(epoch);
      v4u qg;
      for (unsigned spins = 0; spins < SPIN_CAP; ++spins) {
        MEMB();
        qg = __builtin_amdgcn_raw_buffer_load_b128(rws, WS_QG + head * 1024 + lane * 16, 0, AUX_SC1);
        if (__all(TOK(qg))) break;
        __builtin_amdgcn_s_sleep(4);
      }
      ldsu[L_Q + lane] = (qg.x & 0xffffu) | (qg.z << 16);
    }
    STAMP(4);
    wg_barrier();
    STAMP(5);
    lds[L_O + w * 128 + lane] = 0.f; lds[L_O + w * 128 + 64 + lane] = 0.f;
    if (lane == 0) { lds[L_ML + 2 * w] = -3.0e38f; lds[L_ML + 2 * w + 1] = 0.f; }
  }
  if (w >= 4) {
    const __amdgpu_buffer_rsrc_t rwo = __builtin_amdgcn_make_buffer_rsrc((void*)p.wo, 0, 4096 * 512 * 2, 0x00020000);
    const int v = w - 4;
    PINM(epoch);
    TAGS(epoch);
    if (w == 4 && b < 64) {
      const v4u sent = {SENT, SENT, SENT, SENT};
#pragma unroll
      for (int i = 0; i < 12; ++i) __builtin_amdgcn_raw_buffer_store_b128(sent, rws, WS_MB1 + b * 12288 + i * 1024 + lane * 16, 0, AUX_SC1);
      if (b < NREP2) __builtin_amdgcn_raw_buffer_store_b128(sent, rws, WS_MB2 + b * 1024 + lane * 16, 0, AUX_SC1);
      DRAIN();
      const v2u gr = {tag_lo, tag_hi};
      if (lane == 0) __builtin_amdgcn_raw_buffer_store_b64(gr, rws, WS_RG + b * 8, 0, AUX_SC1);
    }
    if (split == 2) {
      float a[8] = {0, 0, 0, 0, 0, 0, 0, 0};
      for (int i0 = 0; i0 < 16; i0 += 8) {
        v4u u[8]; unsigned cc[8];
#pragma unroll
        for (int i = 0; i < 8; ++i) {
          const int row = 128 * v + 8 * (i0 + i) + rg;
          u[i] = __builtin_amdgcn_raw_buffer_load_b128(rmod, row * 6144 + 2048 + head * 128 + cg * 16, 0, P_WAUX); cc[i] = p.c[row];
        }
#pragma unroll
        for (int i = 0; i < 8; ++i) {
          const float sc = silu_b(bf2f(cc[i]));
#pragma unroll
          for (int k = 0; k < 4; ++k) { a[2 * k] = fmaf(sc, lo16(u[i][k]), a[2 * k]); a[2 * k + 1] = fmaf(sc, hi16(u[i][k]), a[2 * k + 1]); }
        }
      }
      float z; RED8(a, z);
      lds[L_RED + v * 64 + 8 * cg + col3] = z;
      asm volatile("s_waitcnt lgkmcnt(0)" ::: "memory");
      unsigned o_ = 0; if (lane == 0) o_ = __hip_atomic_fetch_add(ldsu + L_FLAG + 4, 1u, __ATOMIC_RELAXED, __HIP_MEMORY_SCOPE_WORKGROUP);
      if (__builtin_amdgcn_readfirstlane(o_) == 3u && lane < 32) {
        const v2f s = (*(const LAS v2f*)&lds[L_RED + 2 * lane] + *(const LAS v2f*)&lds[L_RED + 64 + 2 * lane]) + (*(const LAS v2f*)&lds[L_RED + 128 + 2 * lane] + *(const LAS v2f*)&lds[L_RED + 192 + 2 * lane]);
        const v4u gr = {tag_lo | bfbits(s.x), tag_hi, tag_lo | bfbits(s.y), tag_hi};
        __builtin_amdgcn_raw_buffer_store_b128(gr, rws, WS_MG + head * 512 + lane * 16, 0, AUX_SC1);
      }
    }
#pragma unroll
    for (int i0 = 0; i0 < 16; i0 += 8) {
      v4u u[8];
#pragma unroll
      for (int i = 0; i < 8; ++i) u[i] = __builtin_amdgcn_raw_buffer_load_b128(rwo, lane * 16 + (i & 3) * 1024, (head * 128 + v * 16 + i0 + (i & 4)) * 1024, P_WOAUX);
#pragma unroll
      for (int i = 0; i < 8; ++i) *(LAS v4u*)&lds[L_WO + (v * 16 + i0 + i) * 256 + lane * 4] = u[i];
      SB();
    }
    asm volatile("s_waitcnt lgkmcnt(0)" ::: "memory");
    if (lane == 0) ((volatile LAS unsigned*)lds)[L_FLAG + v] = 1u;
#pragma unroll
    for (int i0 = 0; i0 < 16; i0 += 8) {
      v4u u[8];
#pragma unroll
      for (int i = 0; i < 8; ++i) u[i] = __builtin_amdgcn_raw_buffer_load_b128(rwo, lane * 16 + (i & 3) * 1024, (head * 128 + w * 16 + i0 + (i & 4)) * 1024, P_WOAUX);
#pragma unroll
      for (int i = 0; i < 8; ++i)
#pragma unroll
        for (int k = 0; k < 4; ++k) { wf[(i0 + i) * 8 + 2 * k] = lo16(u[i][k]); wf[(i0 + i) * 8 + 2 * k + 1] = hi16(u[i][k]); }
#pragma unroll
      for (int i = 0; i < 64; ++i) asm volatile("" : "+v"(wf[i0 * 8 + i]));
      SB();
    }
#pragma unroll
    for (int i = 0; i < 3; ++i) { const int f = min(t + 512 * i, 1375); m1[i] = p.w1[f]; m2[i] = p.b1[f]; m3[i] = p.w2[f]; }
    STAMP(9);
    if (w == 7) {
      const unsigned xr = p.x[8 * (b & 63) + cg];
      for (unsigned spins = 0; spins < SPIN_CAP; ++spins) {
        v4u sg[4], cb_[4], gb_; v2u ga_, rg_;
        MEMB();
#pragma unroll
        for (int i = 0; i < 4; ++i) {
          sg[i] = __builtin_amdgcn_raw_buffer_load_b128(rws, WS_MG + (512 + 8 * lane + 2 * i) * 8, 0, AUX_SC1);
          cb_[i] = __builtin_amdgcn_raw_buffer_load_b128(rws, WS_MG + (1024 + 8 * lane + 2 * i) * 8, 0, AUX_SC1);
        }
        ga_ = __builtin_amdgcn_raw_buffer_load_b64(rws, WS_MG + (8 * (b & 63) + cg) * 8, 0, AUX_SC1);
        gb_ = __builtin_amdgcn_raw_buffer_load_b128(rws, WS_MG + (1536 + 2 * b) * 8, 0, AUX_SC1);
        rg_ = __builtin_amdgcn_raw_buffer_load_b64(rws, WS_RG + lane * 8, 0, AUX_SC1);
        bool ok = (ga_.y == tag_hi) & ((ga_.x & 0xffff0000u) == tag_lo) & (rg_.y == tag_hi) & (rg_.x == tag_lo) & TOK(gb_);
#pragma unroll
        for (int i = 0; i < 4; ++i) ok = ok & TOK(sg[i]) & TOK(cb_[i]);
        *(LAS v4f*)&lds[L_SB + 8 * lane] = v4f{lo16(sg[0].x), lo16(sg[0].z), lo16(sg[1].x), lo16(sg[1].z)};
        *(LAS v4f*)&lds[L_SB + 8 * lane + 4] = v4f{lo16(sg[2].x), lo16(sg[2].z), lo16(sg[3].x), lo16(sg[3].z)};
        *(LAS v4f*)&lds[L_CB + 8 * lane] = v4f{rb(1.f + lo16(cb_[0].x)), rb(1.f + lo16(cb_[0].z)), rb(1.f + lo16(cb_[1].x)), rb(1.f + lo16(cb_[1].z))};
        *(LAS v4f*)&lds[L_CB + 8 * lane + 4] = v4f{rb(1.f + lo16(cb_[2].x)), rb(1.f + lo16(cb_[2].z)), rb(1.f + lo16(cb_[3].x)), rb(1.f + lo16(cb_[3].z))};
        if (lane < 8) { lds[L_SM + lane] = lo16(ga_.x); lds[L_SM + 8 + lane] = bf2f(xr); }
        if (lane == 0) *(LAS v2f*)&lds[L_SM + 16] = v2f{lo16(gb_.x), lo16(gb_.z)};
        if (__all(ok)) break;
        __builtin_amdgcn_s_sleep(127);
      }
    }
    STAMP(10);
  }
  float f1[3], f2[3], f3[3];
#pragma unroll
  for (int i = 0; i < 3; ++i) { f1[i] = bf2f(m1[i]); f2[i] = bf2f(m2[i]); f3[i] = (t + 512 * i < 1376) ? bf2f(m3[i]) : 0.f; }
#pragma unroll
  for (int i = 0; i < 3; ++i) { asm volatile("" : "+v"(f1[i]), "+v"(f2[i]), "+v"(f3[i])); }
  wg_barrier();
  float Mw, Lw;
  {
    float y[8];
    const v2f mlc = *(const LAS v2f*)&lds[L_ML + 2 * cg];
    const float ma = lds[L_ML + 2 * r], mb = lds[L_ML + 8 + 2 * r];
    const int k_ = 16 * w + c, oi = ((k_ >> 2) & 1) * 64 + (k_ & 3) * 16 + (k_ >> 3);
    const float oa = lds[L_O + r * 128 + oi], ob = lds[L_O + 512 + r * 128 + oi];
    Mw = fmaxf(mlc.x, dpp<XOR1>(mlc.x)); Mw = fmaxf(Mw, dpp<XOR2>(Mw)); Mw = fmaxf(Mw, dpp<XOR7>(Mw));
    const float os = xrow_sum(fmaf(oa, ex2((ma - Mw) * LOG2E), ob * ex2((mb - Mw) * LOG2E)));
    Lw = ex2((mlc.x - Mw) * LOG2E) * mlc.y; Lw += dpp<XOR1>(Lw); Lw += dpp<XOR2>(Lw); Lw += dpp<XOR7>(Lw);
#pragma unroll
    for (int k = 0; k < 8; ++k) y[k] = 0.f;
#pragma unroll
    for (int i = 0; i < 16; ++i) {
      const float sk = __builtin_bit_cast(float, __builtin_amdgcn_readlane(__builtin_bit_cast(int, os), i));
#pragma unroll
      for (int k = 0; k < 8; ++k) y[k] = fmaf(sk, wf[i * 8 + k], y[k]);
    }
    *(LAS v4f*)&lds[L_Y + w * 512 + 8 * lane] = v4f{y[0], y[1], y[2], y[3]};
    *(LAS v4f*)&lds[L_Y + w * 512 + 8 * lane + 4] = v4f{y[4], y[5], y[6], y[7]};
  }
  wg_barrier();
  STAMP(11);
  if (w < 3) {
    const int rd = t / 3, part = t - 3 * rd;
    v4f s = *(const LAS v4f*)&lds[L_Y + 8 * rd + 4 * (part & 1)];
#pragma unroll
    for (int v = 1; v < 8; ++v) s += *(const LAS v4f*)&lds[L_Y + v * 512 + 8 * rd + 4 * (part & 1)];
    if (part == 2) s = v4f{Mw, Lw, 0.f, 0.f};
    const v4u msg = {nosent(s.x), nosent(s.y), nosent(s.z), nosent(s.w)};
    __builtin_amdgcn_raw_buffer_store_b128(msg, rws, WS_MB1 + rd * 12288 + (head * 8 + split) * 48 + part * 16, 0, AUX_SC1);
  }
  if (w >= 4 && b < 64) {
    v4u ms[3];
    for (unsigned spins = 0; spins < SPIN_CAP; ++spins) {
      MEMB();
#pragma unroll
      for (int i = 0; i < 3; ++i) ms[i] = __builtin_amdgcn_raw_buffer_load_b128(rws, WS_MB1 + b * 12288 + (t - 256) * 48 + i * 16, 0, AUX_SC1);
      if (__all(NSENT(ms[0]) & NSENT(ms[1]) & NSENT(ms[2]))) break;
    }
    STAMP(12);
    const float mm = __uint_as_float(ms[2].x);
    float M = fmaxf(mm, dpp<XOR1>(mm)); M = fmaxf(M, dpp<XOR2>(M)); M = fmaxf(M, dpp<XOR7>(M));
    const float e = ex2((mm - M) * LOG2E);
    float L = e * __uint_as_float(ms[2].y); L += dpp<XOR1>(L); L += dpp<XOR2>(L); L += dpp<XOR7>(L);
    const float sc = e * rcp(L);
    float a[8];
#pragma unroll
    for (int k = 0; k < 4; ++k) { a[k] = sc * __uint_as_float(ms[0][k]); a[4 + k] = sc * __uint_as_float(ms[1][k]); }
    float z; RED8(a, z);
    z += dpp<XOR1>(z); z += dpp<XOR2>(z); z += dpp<XOR7>(z);
    if (cg == 0) lds[L_RED + (w - 4) * 8 + col3] = z;
    asm volatile("s_waitcnt lgkmcnt(0)" ::: "memory");
    unsigned o_ = 0; if (lane == 0) o_ = __hip_atomic_fetch_add(ldsu + L_FLAG + 5, 1u, __ATOMIC_RELAXED, __HIP_MEMORY_SCOPE_WORKGROUP);
    if (__builtin_amdgcn_readfirstlane(o_) == 3u) {
      const float at = (lds[L_RED + cg] + lds[L_RED + 8 + cg]) + (lds[L_RED + 16 + cg] + lds[L_RED + 24 + cg]);
      const unsigned xb = bfbits(lds[L_SM + 8 + cg] + rb(lds[L_SM + cg] * rb(at)));
      unsigned pk = xb | ((unsigned)__builtin_amdgcn_mov_dpp((int)xb, XOR1, 0xf, 0xf, true) << 16);
      if (pk == SENT) pk = 0x7FC07FC0u;
      const v4u msg = {(unsigned)__builtin_amdgcn_readlane((int)pk, 0), (unsigned)__builtin_amdgcn_readlane((int)pk, 2), (unsigned)__builtin_amdgcn_readlane((int)pk, 4), (unsigned)__builtin_amdgcn_readlane((int)pk, 6)};
      if (lane < NREP2) __builtin_amdgcn_raw_buffer_store_b128(msg, rws, WS_MB2 + lane * 1024 + b * 16, 0, AUX_SC1);
    }
    STAMP(13);
  }
  if (w == 7) {
    float sb[8], cb[8];
    { const v4f a0 = *(const LAS v4f*)&lds[L_SB + 8 * lane], a1 = *(const LAS v4f*)&lds[L_SB + 8 * lane + 4], c0 = *(const LAS v4f*)&lds[L_CB + 8 * lane], c1 = *(const LAS v4f*)&lds[L_CB + 8 * lane + 4];
#pragma unroll
      for (int i = 0; i < 4; ++i) { sb[i] = a0[i]; sb[4 + i] = a1[i]; cb[i] = c0[i]; cb[4 + i] = c1[i]; } }
    v4u xs;
    for (unsigned spins = 0; spins < SPIN_CAP; ++spins) {
      MEMB();
      xs = __builtin_amdgcn_raw_buffer_load_b128(rws, WS_MB2 + (b % NREP2) * 1024 + lane * 16, 0, AUX_SC1);
      if (__all(NSENT(xs))) break;
    }
    STAMP(14);
    float v[8];
#pragma unroll
    for (int i = 0; i < 4; ++i) { v[2 * i] = lo16(xs[i]); v[2 * i + 1] = hi16(xs[i]); }
    ln8(v);
#pragma unroll
    for (int i = 0; i < 8; ++i) v[i] = rb(rb(v[i] * cb[i]) + sb[i]);
    ln8(v);
    const int e_ = b & 3;
    const unsigned xw = e_ == 0 ? xs.x : e_ == 1 ? xs.y : e_ == 2 ? xs.z : xs.w;
    const float ha = e_ == 0 ? v[0] : e_ == 1 ? v[2] : e_ == 2 ? v[4] : v[6], hb = e_ == 0 ? v[1] : e_ == 1 ? v[3] : e_ == 2 ? v[5] : v[7];
    if (lane == (b >> 2)) { *(LAS v2f*)&lds[L_SM + 18] = v2f{lo16(xw), hi16(xw)}; *(LAS v2f*)&lds[L_SM + 20] = v2f{ha, hb}; }
    if (b == 0 && lane == 0) __hip_atomic_store((GAS u64*)(p.ws + WS_EPOCH), epoch + 1, RLX_AGENT);
  }
  wg_barrier();
  STAMP(15);
  {
    const v2f h2 = *(const LAS v2f*)&lds[L_SM + 20];
    float s0 = 0.f, s1 = 0.f;
#pragma unroll
    for (int i = 0; i < 3; ++i) {
#pragma unroll
      for (int d = 0; d < 2; ++d) {
        const float u = rb(rb(h2[d] * f1[i]) + f2[i]);
        const float z = u * fmaf(0.044715f * u, u, 1.f) * (-2.f * 0.7978845608028654f * LOG2E);
        const float gl = rb(u * rcp(1.f + ex2(z)));
        if (d == 0) s0 = fmaf(gl, f3[i], s0); else s1 = fmaf(gl, f3[i], s1);
      }
    }
    s0 = row_sum(s0); s1 = row_sum(s1);
    const float u = sw16_sum(s0, s1), z = sw32_sum(u, u);
    if (lane == 0 || lane == 16) lds[L_FIN + w * 2 + (lane >> 4)] = z;
  }
  wg_barrier();
  if (t < 2) {
    float s = 0.f;
#pragma unroll
    for (int v = 0; v < 8; ++v) s += lds[L_FIN + v * 2 + t];
    const float yv = rb(rb(s) + bf2f(b2v));
    const float o = lds[L_SM + 18 + t] + rb(lds[L_SM + 16 + t] * yv);
    p.out[2 * b + t] = (us)bfbits(o);
  }
  STAMP(21);
}

extern "C" void kernel_launch(void* const* d_in, const int* in_sizes, int n_in, void* d_out, int out_size, void* d_ws, size_t ws_size, hipStream_t stream) {
  Params p;
  p.x = (const us*)d_in[0]; p.K = (const us*)d_in[1]; p.V = (const us*)d_in[2]; p.wq = (const us*)d_in[3]; p.wo = (const us*)d_in[4];
  p.w1 = (const us*)d_in[6]; p.b1 = (const us*)d_in[7]; p.w2 = (const us*)d_in[8]; p.b2 = (const us*)d_in[9];
  p.c = (const us*)d_in[10]; p.wmod = (const us*)d_in[11];
  p.out = (us*)d_out; p.ws = (unsigned char*)d_ws;
  fused_kernel<<<256, 512, 0, stream>>>(p);
}
```

```cpp
#include <hip/hip_runtime.h>
#include <stdint.h>

typedef unsigned v4u __attribute__((ext_vector_type(4)));
typedef unsigned v2u __attribute__((ext_vector_type(2)));
typedef float v4f __attribute__((ext_vector_type(4)));
typedef float v2f __attribute__((ext_vector_type(2)));
typedef __bf16 bf16x2 __attribute__((ext_vector_type(2)));
typedef unsigned short us;
typedef unsigned long long u64;
#define GAS __attribute__((address_space(1)))
#define LAS __attribute__((address_space(3)))
#define RLX_AGENT __ATOMIC_RELAXED, __HIP_MEMORY_SCOPE_AGENT

#define P_NMT 49
#define P_S0 ((512 - 6 * (P_NMT + 18)) / 2 + 1)
#define P_ROT 5
#define P_NREP2 8
#define P_NREPS 4
#define P_PACE "7"
#define P_WQP "15"
#define P_PACES "1"
#define P_NSLOW 0
#define P_GDLY 3
#define DBG_STAMPS 0
constexpr int NMT = P_NMT, NX = 6, SZ_O = NMT + 3 * NX, TW = (2 * NMT + 3 * NX) / 4;
constexpr int SZ_S0 = P_S0, SZ_S4 = 512 - 6 * SZ_O - SZ_S0;
constexpr int NREP2 = P_NREP2, NREPS = P_NREPS;
static_assert(TW - 2 * ((NMT + 3) / 4) >= 3 && TW - 2 * (NMT / 4) <= 5 && (2 * NMT + 3 * NX) % 4 == 0 && NMT >= 28 && SZ_S0 >= 28 && SZ_S4 >= 28 && SZ_S0 <= 128 && SZ_S4 <= 128 && SZ_O <= 128, "split");
static_assert(NREP2 <= 32 && (NREPS == 2 || NREPS == 4 || NREPS == 8), "knobs");
constexpr unsigned SPIN_CAP = 1u << 18;

constexpr int WS_EPOCH = 0;
constexpr int WS_RG = 4096;
constexpr int WS_ML = 8192;
constexpr int WS_MG = 16384;
constexpr int WS_QG = 32768;
constexpr int WS_SG = 65536;
constexpr int WS_YG = 327680;
constexpr int WS_MB2 = 458752;
constexpr int WS_OG = 524288;
constexpr int WS_END = WS_OG + (256 << 10);
constexpr int WS_DBG = 2 << 20;
constexpr unsigned SENT = 0xFFFFFFFFu;
constexpr int AUX_NT = 2, AUX_SC1 = 16;

constexpr int L_RED = 0;
constexpr int L_SM = 512;
constexpr int L_Q = 560;
constexpr int L_ML = 624;
constexpr int L_O = 640;
constexpr int L_LN = 1152;
constexpr int L_SC = L_LN + 2048;
constexpr int L_SB = L_SC + 4096;
constexpr int L_CB = L_SB + 512;
constexpr int L_FIN = L_CB + 512;
constexpr int L_FLAG = L_FIN + 16;
constexpr int L_TOT = L_FLAG + 16;

struct Params {
  const us *x, *K, *V, *wq, *wo, *w1, *b1, *w2, *b2, *c, *wmod;
  us* out;
  unsigned char* ws;
};

__device__ __forceinline__ float bf2f(unsigned b) { return __uint_as_float(b << 16); }
__device__ __forceinline__ float lo16(unsigned u) { return __uint_as_float(u << 16); }
__device__ __forceinline__ float hi16(unsigned u) { return __uint_as_float(u & 0xffff0000u); }
__device__ __forceinline__ float rb(float f) { return (float)(__bf16)f; }
__device__ __forceinline__ unsigned bfbits(float f) { return (unsigned)__builtin_bit_cast(us, (__bf16)f); }
__device__ __forceinline__ float dot2(unsigned k, unsigned q, float acc) {
  return __builtin_amdgcn_fdot2_f32_bf16(__builtin_bit_cast(bf16x2, k), __builtin_bit_cast(bf16x2, q), acc, false);
}
template <int CTRL> __device__ __forceinline__ float dpp(float x) {
  return __builtin_bit_cast(float, __builtin_amdgcn_mov_dpp(__builtin_bit_cast(int, x), CTRL, 0xf, 0xf, true));
}
constexpr int XOR1 = 0xB1, XOR2 = 0x4E, XOR7 = 0x141, XOR8 = 0x128, BC = 0x150;
__device__ __forceinline__ float sw16_sum(float a, float b) {
  auto s = __builtin_amdgcn_permlane16_swap(__float_as_uint(a), __float_as_uint(b), false, false);
  return __uint_as_float(s[0]) + __uint_as_float(s[1]);
}
__device__ __forceinline__ float sw32_sum(float a, float b) {
  auto s = __builtin_amdgcn_permlane32_swap(__float_as_uint(a), __float_as_uint(b), false, false);
  return __uint_as_float(s[0]) + __uint_as_float(s[1]);
}
__device__ __forceinline__ float xrow_max(float x) {
  auto s = __builtin_amdgcn_permlane16_swap(__float_as_uint(x), __float_as_uint(x), false, false);
  x = fmaxf(__uint_as_float(s[0]), __uint_as_float(s[1]));
  auto t = __builtin_amdgcn_permlane32_swap(__float_as_uint(x), __float_as_uint(x), false, false);
  return fmaxf(__uint_as_float(t[0]), __uint_as_float(t[1]));
}
__device__ __forceinline__ float xrow_sum(float x) { x = sw16_sum(x, x); return sw32_sum(x, x); }
__device__ __forceinline__ float row_sum(float x) { x += dpp<XOR1>(x); x += dpp<XOR2>(x); x += dpp<XOR7>(x); x += dpp<XOR8>(x); return x; }
__device__ __forceinline__ float wave_sum(float x) { return xrow_sum(row_sum(x)); }
__device__ __forceinline__ float ex2(float x) { return __builtin_amdgcn_exp2f(x); }
__device__ __forceinline__ float rcp(float x) { return __builtin_amdgcn_rcpf(x); }
__device__ __forceinline__ float rdl(float x, int l) { return __builtin_bit_cast(float, __builtin_amdgcn_readlane(__builtin_bit_cast(int, x), l)); }
constexpr float LOG2E = 1.4426950408889634f;
#define PINM(x) asm volatile("" : "+v"(x) :: "memory")
#define SB() __builtin_amdgcn_sched_barrier(0)
__device__ __forceinline__ void wg_barrier() {
  asm volatile("s_waitcnt lgkmcnt(0)\n\ts_barrier" ::: "memory");
}
__device__ __forceinline__ unsigned nosent(float f) { unsigned u = __float_as_uint(f); return u == SENT ? 0x7FC00000u : u; }
__device__ __forceinline__ void ln8(float (&v)[8]) {
  const float sh = __builtin_bit_cast(float, __builtin_amdgcn_readfirstlane(__builtin_bit_cast(int, v[0])));
  float s = 0.f, q = 0.f;
#pragma unroll
  for (int i = 0; i < 8; ++i) { const float d = v[i] - sh; s += d; q = fmaf(d, d, q); }
  s = row_sum(s); q = row_sum(q);
  const float u = sw16_sum(s, q), z = sw32_sum(u, u);
  const float ms = rdl(z, 0) * (1.f / 512.f), mq = rdl(z, 16) * (1.f / 512.f);
  const float var = fmaxf(fmaf(-ms, ms, mq), 0.f);
  const float mb = rb(sh + ms), rden = 1.f / rb(__builtin_amdgcn_sqrtf(rb(rb(var) + 1e-5f)));
#pragma unroll
  for (int i = 0; i < 8; ++i) v[i] = rb(rb(v[i] - mb) * rden);
}
__device__ __forceinline__ unsigned f24(float f) { const unsigned u = __float_as_uint(f); return ((u & 0x7f800000u) == 0x7f800000u ? u : u + 0x80u) >> 8; }
__device__ __forceinline__ float silu_b(float v) { return rb(v / (1.f + __expf(-v))); }

#define STAMP(i) do { } while (0)

__global__ void __launch_bounds__(512, 2) fused_kernel(Params p) {
  __shared__ __attribute__((aligned(16))) float lds[L_TOT];
  LAS unsigned* ldsu = (LAS unsigned*)lds;
  const int t = threadIdx.x, lane = t & 63, w = __builtin_amdgcn_readfirstlane(t >> 6), v = w & 3;
  const int r = lane >> 4, c = lane & 15, rg = lane >> 3, cg = lane & 7;
  const int colx = 8 * cg + ((lane >> 3) & 1) * 4 + ((lane >> 4) & 1) * 2 + (lane >> 5);
  const int b = blockIdx.x, xcd = b & 7, j = b >> 3;
  const int head = xcd * 4 + (j >> 3), split = j & 7;
  const bool svc = (split & 3) == 0;
  const __amdgpu_buffer_rsrc_t rws = __builtin_amdgcn_make_buffer_rsrc((void*)p.ws, 0, WS_END, 0x00020000);
  const __amdgpu_buffer_rsrc_t rmod = __builtin_amdgcn_make_buffer_rsrc((void*)p.wmod, 0, 512 * 3072 * 2, 0x00020000);
  STAMP(0);
#define TAGS(e) const unsigned tag_hi = (unsigned)(((e) + 1) >> 16), tag_lo = (unsigned)((e) + 1) << 16
#define TAGF(e) const unsigned tgf_hi = (unsigned)(((e) + 1) >> 8), tgf_lo = (unsigned)((e) + 1) << 24
#define TOK(u) ((u.y == tag_hi) & (u.w == tag_hi) & ((u.x & 0xffff0000u) == tag_lo) & ((u.z & 0xffff0000u) == tag_lo))
#define TOKF(u) ((u.y == tgf_hi) & (u.w == tgf_hi) & ((u.x & 0xff000000u) == tgf_lo) & ((u.z & 0xff000000u) == tgf_lo))
#define NSENT(u) ((u.x != SENT) & (u.y != SENT) & (u.z != SENT) & (u.w != SENT))
#define MEMB() asm volatile("" ::: "memory")
#define DRAIN() asm volatile("s_waitcnt vmcnt(0)" ::: "memory")
#define F24(u) __uint_as_float((u) << 8)
#define FMA8(a, s, u) _Pragma("unroll") for (int k = 0; k < 4; ++k) { a[2 * k] = fmaf(s, lo16(u[k]), a[2 * k]); a[2 * k + 1] = fmaf(s, hi16(u[k]), a[2 * k + 1]); }
#define RED_ARRIVE(a, buf, ctr) ({ \
    _Pragma("unroll") for (int h = 0; h < 4; ++h) { const float t1 = a[h] + dpp<XOR8>(a[h]), t2 = a[h + 4] + dpp<XOR8>(a[h + 4]); a[h] = (lane & 8) ? t2 : t1; } \
    const float u0_ = sw16_sum(a[0], a[2]), u1_ = sw16_sum(a[1], a[3]); \
    lds[(buf) + v * 64 + colx] = sw32_sum(u0_, u1_); \
    asm volatile("s_waitcnt lgkmcnt(0)" ::: "memory"); \
    unsigned o_ = 0; if (lane == 0) o_ = __hip_atomic_fetch_add(ldsu + L_FLAG + (ctr), 1u, __ATOMIC_RELAXED, __HIP_MEMORY_SCOPE_WORKGROUP); \
    const bool last_ = __builtin_amdgcn_readfirstlane(o_) == 3u; MEMB(); last_; })
#define SUM4(buf) ({ const int l_ = 2 * (lane & 31); (*(const LAS v2f*)&lds[(buf) + l_] + *(const LAS v2f*)&lds[(buf) + 64 + l_]) + (*(const LAS v2f*)&lds[(buf) + 128 + l_] + *(const LAS v2f*)&lds[(buf) + 192 + l_]); })
#define LDSWAIT(idx, val) do { for (unsigned spins = 0; spins < SPIN_CAP * 16; ++spins) { if (((volatile LAS unsigned*)lds)[L_FLAG + (idx)] >= (val)) break; __builtin_amdgcn_s_sleep(1); } MEMB(); } while (0)
#define LDSSET(idx) do { asm volatile("s_waitcnt lgkmcnt(0)" ::: "memory"); if (lane == 0) ((volatile LAS unsigned*)lds)[L_FLAG + (idx)] = 1u; } while (0)
  if (t < 8) ldsu[L_FLAG + t] = 0u;
  const int g = head * 2 + (split >> 2);

  u64 epoch;
  if (svc && w < 4) {
    const int lc = g & 15, ks = g >> 4;
    v4u wa[4]; unsigned cv[4];
#pragma unroll
    for (int i = 0; i < 4; ++i) wa[i] = __builtin_amdgcn_raw_buffer_load_b128(rmod, (128 * ks + 32 * v + 8 * i + rg) * 6144 + lc * 128 + cg * 16, 0, 0);
#pragma unroll
    for (int i = 0; i < 4; ++i) cv[i] = *(const GAS unsigned*)(p.c + 128 * ks + 32 * v + 8 * i + (rg & 6));
    epoch = __hip_atomic_load((GAS u64*)(p.ws + WS_EPOCH), RLX_AGENT);
    SB();
    wg_barrier();
    PINM(cv[0]); PINM(cv[1]); PINM(cv[2]); PINM(cv[3]);
    float a[8] = {0, 0, 0, 0, 0, 0, 0, 0};
#pragma unroll
    for (int i = 0; i < 4; ++i) { const float sc = silu_b((rg & 1) ? hi16(cv[i]) : lo16(cv[i])); FMA8(a, sc, wa[i]); }
    STAMP(16);
    if (RED_ARRIVE(a, L_RED, 0)) {
      TAGF(epoch);
      const v2f s = SUM4(L_RED);
      const v4u gr = {tgf_lo | f24(s.x), tgf_hi, tgf_lo | f24(s.y), tgf_hi};
#pragma unroll
      for (int rep = 0; rep < NREPS; rep += 2) __builtin_amdgcn_raw_buffer_store_b128(gr, rws, WS_SG + (rep + (lane >> 5)) * 32768 + (ks * 1024 + lc * 64) * 8 + (lane & 31) * 16, 0, AUX_SC1);
    }
    STAMP(17);
  } else {
    epoch = __hip_atomic_load((GAS u64*)(p.ws + WS_EPOCH), RLX_AGENT);
  }
  if (svc && w >= 4) {
    const __amdgpu_buffer_rsrc_t rwq = __builtin_amdgcn_make_buffer_rsrc((void*)p.wq, 0, 512 * 4096 * 2, 0x00020000);
    const v4u xv = *(const GAS v4u*)(p.x + lane * 8);
    SB();
    wg_barrier();
    v4u wq[16];
#pragma unroll
    for (int i = 0; i < 16; ++i) { asm volatile("s_waitcnt vmcnt(" P_WQP ")" ::: "memory"); wq[i] = __builtin_amdgcn_raw_buffer_load_b128(rwq, (128 * v + 8 * i + rg) * 8192 + g * 128 + cg * 16, 0, 0); SB(); }
    SB();
    v2f ln;
    {
      float xx[8];
#pragma unroll
      for (int i = 0; i < 4; ++i) { xx[2 * i] = lo16(xv[i]); xx[2 * i + 1] = hi16(xv[i]); }
      ln8(xx);
      *(LAS v4f*)&lds[L_LN + v * 512 + lane * 8] = v4f{xx[0], xx[1], xx[2], xx[3]};
      *(LAS v4f*)&lds[L_LN + v * 512 + lane * 8 + 4] = v4f{xx[4], xx[5], xx[6], xx[7]};
      ln = *(const LAS v2f*)&lds[L_LN + v * 512 + 128 * v + 2 * lane];
    }
    PINM(epoch);
    TAGS(epoch); TAGF(epoch);
    const int so = WS_SG + (g & (NREPS - 1)) * 32768 + (128 * v + 2 * lane) * 8;
    v4u gs[4], gc[4];
    bool ok = false;
    for (unsigned spins = 0; spins < SPIN_CAP; ++spins) {
      MEMB();
      if (!ok) {
#pragma unroll
        for (int i = 0; i < 4; ++i) { gs[i] = __builtin_amdgcn_raw_buffer_load_b128(rws, so + i * 8192, 0, AUX_SC1); gc[i] = __builtin_amdgcn_raw_buffer_load_b128(rws, so + i * 8192 + 4096, 0, AUX_SC1); }
        ok = TOKF(gs[0]) & TOKF(gs[1]) & TOKF(gs[2]) & TOKF(gs[3]) & TOKF(gc[0]) & TOKF(gc[1]) & TOKF(gc[2]) & TOKF(gc[3]);
      }
      if (__all(ok)) break;
    }
    STAMP(18);
#define S4X(a) rb((F24(a[0].x) + F24(a[1].x)) + (F24(a[2].x) + F24(a[3].x)))
#define S4Z(a) rb((F24(a[0].z) + F24(a[1].z)) + (F24(a[2].z) + F24(a[3].z)))
    const float h0 = rb(rb(ln.x * rb(1.f + S4X(gc))) + S4X(gs)), h1 = rb(rb(ln.y * rb(1.f + S4Z(gc))) + S4Z(gs));
    const unsigned hp = bfbits(h0) | (bfbits(h1) << 16);
    float a[8] = {0, 0, 0, 0, 0, 0, 0, 0};
#pragma unroll
    for (int i = 0; i < 16; ++i) {
      const unsigned hs = (unsigned)__shfl((int)hp, 4 * i + (rg >> 1));
      const float hh = (rg & 1) ? hi16(hs) : lo16(hs);
      FMA8(a, hh, wq[i]);
    }
    STAMP(19);
    if (RED_ARRIVE(a, L_RED + 256, 1) && lane < 32) {
      const v2f s = SUM4(L_RED + 256);
      const v4u gr = {tag_lo | bfbits(s.x), tag_hi, tag_lo | bfbits(s.y), tag_hi};
      __builtin_amdgcn_raw_buffer_store_b128(gr, rws, WS_QG + g * 512 + lane * 16, 0, AUX_SC1);
    }
    STAMP(20);
  }

  unsigned wu[16], m1[3], m2[3], m3[3], b2u, tgf_hi, tgf_lo;
  float wf[16], f1[3], f2[3], f3[3], b2f;
#define LOADW() do { \
    _Pragma("unroll") for (int i = 0; i < 16; ++i) wu[i] = p.wo[(head * 128 + 64 * (w & 1) + 8 * (i >> 1) + (w >> 1) + 4 * (i & 1)) * 512 + split * 64 + lane]; \
    _Pragma("unroll") for (int i = 0; i < 3; ++i) { const int f = min(t + 512 * i, 1375); m1[i] = p.w1[f]; m2[i] = p.b1[f]; m3[i] = p.w2[f]; } \
    b2u = p.b2[0]; SB(); } while (0)
#define CONVW() do { \
    _Pragma("unroll") for (int i = 0; i < 16; ++i) { wf[i] = bf2f(wu[i]); asm volatile("" : "+v"(wf[i])); } \
    _Pragma("unroll") for (int i = 0; i < 3; ++i) { f1[i] = bf2f(m1[i]); f2[i] = bf2f(m2[i]); f3[i] = (t + 512 * i < 1376) ? bf2f(m3[i]) : 0.f; asm volatile("" : "+v"(f1[i]), "+v"(f2[i]), "+v"(f3[i])); } \
    b2f = bf2f(b2u); tgf_hi = (unsigned)((epoch + 1) >> 8); tgf_lo = (unsigned)(epoch + 1) << 24; \
    asm volatile("" : "+v"(tgf_hi), "+v"(tgf_lo), "+v"(b2f)); SB(); } while (0)

  const bool streamer = w < 4 || (w > 4 && !svc);
  v4u R[8];
  float acc[8] = {0, 0, 0, 0, 0, 0, 0, 0};
  float pp = 0.f;
#define PB(i) ((i) == 0 ? dpp<BC + 0>(pp) : (i) == 1 ? dpp<BC + 2>(pp) : (i) == 2 ? dpp<BC + 4>(pp) : (i) == 3 ? dpp<BC + 6>(pp) : (i) == 4 ? dpp<BC + 8>(pp) : (i) == 5 ? dpp<BC + 10>(pp) : (i) == 6 ? dpp<BC + 12>(pp) : dpp<BC + 14>(pp))
#define LDU(rs, i, base) __builtin_amdgcn_raw_buffer_load_b128(rs, lane * 16 + ((i) & 3) * 1024, (base) + ((i) >> 2) * 4096, AUX_NT)
#define CONSV(B, REFILL, rs, base) do { \
    _Pragma("unroll") for (int i = 0; i < 8; ++i) { const float pi = PB(i); FMA8(acc, pi, B[i]); \
      if (REFILL) { PINM(acc[0]); PINM(acc[1]); PINM(acc[2]); PINM(acc[3]); PINM(acc[4]); PINM(acc[5]); PINM(acc[6]); PINM(acc[7]); B[i] = LDU(rs, i, base); SB(); } } } while (0)
  if (streamer) {
    const __amdgpu_buffer_rsrc_t rk = __builtin_amdgcn_make_buffer_rsrc((void*)p.K, 0, 128 << 20, 0x00020000);
    const int base = (head << 22) + (((split > 0 ? SZ_S0 - SZ_O : 0) + (split > 4 ? SZ_S4 - SZ_O : 0) + split * SZ_O) << 13);
    const int mt = svc ? (split ? SZ_S4 : SZ_S0) : NMT;
    const int n = w < 4 ? (mt + 3 - w) >> 2 : NX;
    const int u0 = w < 4 ? w * (mt >> 2) + min(w, mt & 3) : NMT + (w - 5) * NX;
    const int ne = svc ? 0 : TW - 2 * n, e0 = NMT + w * TW - 2 * u0;
    const int rot = (head * P_ROT + split * 3 + w) % n;
    LAS float* sc = (LAS float*)&lds[L_SC + r * 8 + (c >> 1)];
#define UK(z) ({ const unsigned i_ = (z) + rot; u0 + (int)min(i_, i_ - (unsigned)n); })
#define UZ(z) ({ const unsigned z_ = (z); const int k_ = UK(min(z_, z_ - (unsigned)n)), x_ = e0 + (int)z_ - 2 * n; (int)z_ < 2 * n ? k_ : x_; })
#define ZRS(z) __builtin_amdgcn_make_buffer_rsrc((void*)((z) < n ? p.K : p.V), 0, 128 << 20, 0x00020000)
#define FILL(B, z) do { const int a_ = base + (UK(z) << 13); _Pragma("unroll") for (int i = 0; i < 8; ++i) { asm volatile("s_waitcnt vmcnt(" P_PACE ")" ::: "memory"); \
      if ((z) < P_NSLOW) asm volatile("s_cmp_eq_u32 %0, 0\n\ts_cbranch_scc1 1f\n\ts_waitcnt vmcnt(" P_PACES ")\n1:" :: "s"(slow) : "scc", "memory"); \
      B[i] = LDU(rk, i, a_); SB(); } } while (0)
    const int slow = __builtin_amdgcn_readfirstlane(svc ? 1 : 0);
    v4u E1[8], E2[8], E3[8], E4[8], E5[8];
    FILL(R, 0);
    if (!svc) wg_barrier();
    FILL(E1, 1); FILL(E2, 2); FILL(E3, 3); FILL(E4, 4); FILL(E5, 5);
    STAMP(4);
    LDSWAIT(7, 1u);
    STAMP(5);
    const v4u q = *(const LAS v4u*)&lds[L_Q + 4 * c];
    float m = -3.0e38f;
#define CONSK(B, z, REFILL, rs, off) do { float d_[8]; \
      _Pragma("unroll") for (int i = 0; i < 8; ++i) { \
        float s_ = dot2(B[i][0], q[0], 0.f); s_ = dot2(B[i][1], q[1], s_); s_ = dot2(B[i][2], q[2], s_); s_ = dot2(B[i][3], q[3], s_); d_[i] = s_; \
        if (REFILL) { PINM(d_[i]); B[i] = LDU(rs, i, off); SB(); } } \
      _Pragma("unroll") for (int h = 0; h < 4; ++h) { const float t1 = d_[h] + dpp<XOR8>(d_[h]), t2 = d_[h + 4] + dpp<XOR8>(d_[h + 4]); d_[h] = (c & 8) ? t2 : t1; } \
      _Pragma("unroll") for (int h = 0; h < 2; ++h) { const float t1 = d_[h] + dpp<XOR7>(d_[h]), t2 = d_[h + 2] + dpp<XOR7>(d_[h + 2]); d_[h] = (c & 4) ? t2 : t1; } \
      { const float t1 = d_[0] + dpp<XOR2>(d_[0]), t2 = d_[1] + dpp<XOR2>(d_[1]); d_[0] = (c & 2) ? t2 : t1; } \
      float s_ = d_[0] + dpp<XOR1>(d_[0]); \
      s_ = rb(rb(s_) * 0.08838834764831845f); \
      m = fmaxf(m, s_); sc[UK(z) * 32] = s_; } while (0)
    if (w < 4) {
      { const int a = base + (UK(6) << 13); CONSK(R, 0, true, rk, a); }
      CONSK(E1, 1, false, rk, 0); CONSK(E2, 2, false, rk, 0); CONSK(E3, 3, false, rk, 0); CONSK(E4, 4, false, rk, 0); CONSK(E5, 5, false, rk, 0);
      SB();
      STAMP(6);
      LOADW();
      int z = 6;
      do {
        const __amdgpu_buffer_rsrc_t rn = ZRS(z + 1);
        const int a = base + (UZ(z + 1) << 13);
        CONSK(R, z, true, rn, a);
      } while (++z < n);
      STAMP(7);
      if (!svc) {
        LDSWAIT(6, 3u);
        const float x0 = sc[e0 * 32], x1 = sc[(e0 + 1) * 32], x2 = sc[(e0 + 2) * 32], x3 = sc[(e0 + min(3, ne - 1)) * 32], x4 = sc[(e0 + ne - 1) * 32];
        m = fmaxf(fmaxf(m, x0), fmaxf(fmaxf(x1, x2), fmaxf(x3, x4)));
      }
      const float s0_ = sc[UZ(n) * 32];
      m = fmaxf(m, dpp<XOR2>(m)); m = fmaxf(m, dpp<XOR7>(m)); m = fmaxf(m, dpp<XOR8>(m)); m = xrow_max(m);
      if (lane == 0) lds[L_ML + w] = m;
      asm volatile("s_waitcnt lgkmcnt(0)" ::: "memory");
      if (lane == 0) (void)__hip_atomic_fetch_add(ldsu + L_FLAG + 5, 1u, __ATOMIC_RELAXED, __HIP_MEMORY_SCOPE_WORKGROUP);
      const __amdgpu_buffer_rsrc_t rv = ZRS(n);
      const int zt = 2 * n + ne;
      pp = ex2((s0_ - m) * LOG2E);
      CONVW();
      for (; z < zt - 1; ++z) {
        const int U = UZ(z + 1);
        const float sn = sc[U * 32];
        CONSV(R, true, rv, base + (U << 13));
        pp = ex2((sn - m) * LOG2E);
      }
      LDSWAIT(3, 1u);
      const float fw = lds[L_ML + 8 + w];
      pp *= fw;
#pragma unroll
      for (int k = 0; k < 8; ++k) acc[k] *= fw;
      STAMP(9);
    } else {
      CONSK(R, 0, false, rk, 0); CONSK(E1, 1, false, rk, 0); CONSK(E2, 2, false, rk, 0); CONSK(E3, 3, false, rk, 0); CONSK(E4, 4, false, rk, 0); CONSK(E5, 5, false, rk, 0);
      asm volatile("s_waitcnt lgkmcnt(0)" ::: "memory");
      if (lane == 0) (void)__hip_atomic_fetch_add(ldsu + L_FLAG + 6, 1u, __ATOMIC_RELAXED, __HIP_MEMORY_SCOPE_WORKGROUP);
    }
  }
  if (w >= 4) {
    if (w == 4 && !svc) wg_barrier();
    PINM(epoch);
    TAGS(epoch);
    if (w == 4) {
      v4u qg;
      for (unsigned spins = 0; spins < SPIN_CAP; ++spins) {
        MEMB();
        qg = __builtin_amdgcn_raw_buffer_load_b128(rws, WS_QG + head * 1024 + lane * 16, 0, AUX_SC1);
        if (__all(TOK(qg))) break;
        __builtin_amdgcn_s_sleep(4);
      }
      ldsu[L_Q + lane] = (qg.x & 0xffffu) | (qg.z << 16);
      LDSSET(7);
      STAMP(5);
    }
    if (w == 5 && b < NREP2) {
      const v4u sent = {SENT, SENT, SENT, SENT};
      __builtin_amdgcn_raw_buffer_store_b128(sent, rws, WS_MB2 + b * 1024 + lane * 16, 0, AUX_SC1);
      DRAIN();
      const v2u gr = {tag_lo, tag_hi};
      if (lane == 0) __builtin_amdgcn_raw_buffer_store_b64(gr, rws, WS_RG + b * 8, 0, AUX_SC1);
    }
    if (split == 4) {
      float a[8] = {0, 0, 0, 0, 0, 0, 0, 0};
      for (int i0 = 0; i0 < 16; i0 += 4) {
        v4u u[4]; unsigned cc[4];
#pragma unroll
        for (int i = 0; i < 4; ++i) {
          const int row = 128 * v + 8 * (i0 + i) + rg;
          u[i] = __builtin_amdgcn_raw_buffer_load_b128(rmod, row * 6144 + 2048 + head * 128 + cg * 16, 0, 0); cc[i] = p.c[row];
        }
#pragma unroll
        for (int i = 0; i < 4; ++i) { const float sc = silu_b(bf2f(cc[i])); FMA8(a, sc, u[i]); }
      }
      if (RED_ARRIVE(a, L_O, 4) && lane < 32) {
        const v2f s = SUM4(L_O);
        const v4u gr = {tag_lo | bfbits(s.x), tag_hi, tag_lo | bfbits(s.y), tag_hi};
        __builtin_amdgcn_raw_buffer_store_b128(gr, rws, WS_MG + head * 512 + lane * 16, 0, AUX_SC1);
      }
    }
    STAMP(10);
    LOADW();
    CONVW();
    if (w == 7) {
      const unsigned xr = p.x[16 * (b & 31) + c];
      for (int i = 0; i < P_GDLY; ++i) __builtin_amdgcn_s_sleep(127);
#define GATHER(AUX) ({ v4u sg[4], cb_[4], gb_; v2u ga_, rg_; MEMB(); \
        _Pragma("unroll") for (int i = 0; i < 4; ++i) { \
          sg[i] = __builtin_amdgcn_raw_buffer_load_b128(rws, WS_MG + (512 + 8 * lane + 2 * i) * 8, 0, AUX); \
          cb_[i] = __builtin_amdgcn_raw_buffer_load_b128(rws, WS_MG + (1024 + 8 * lane + 2 * i) * 8, 0, AUX); } \
        ga_ = __builtin_amdgcn_raw_buffer_load_b64(rws, WS_MG + (16 * (b & 31) + c) * 8, 0, AUX); \
        gb_ = __builtin_amdgcn_raw_buffer_load_b128(rws, WS_MG + (1536 + 2 * b) * 8, 0, AUX); \
        rg_ = __builtin_amdgcn_raw_buffer_load_b64(rws, WS_RG + (lane % NREP2) * 8, 0, AUX); \
        bool ok = (ga_.y == tag_hi) & ((ga_.x & 0xffff0000u) == tag_lo) & (rg_.y == tag_hi) & (rg_.x == tag_lo) & TOK(gb_); \
        _Pragma("unroll") for (int i = 0; i < 4; ++i) ok = ok & TOK(sg[i]) & TOK(cb_[i]); \
        *(LAS v4f*)&lds[L_SB + 8 * lane] = v4f{lo16(sg[0].x), lo16(sg[0].z), lo16(sg[1].x), lo16(sg[1].z)}; \
        *(LAS v4f*)&lds[L_SB + 8 * lane + 4] = v4f{lo16(sg[2].x), lo16(sg[2].z), lo16(sg[3].x), lo16(sg[3].z)}; \
        *(LAS v4f*)&lds[L_CB + 8 * lane] = v4f{rb(1.f + lo16(cb_[0].x)), rb(1.f + lo16(cb_[0].z)), rb(1.f + lo16(cb_[1].x)), rb(1.f + lo16(cb_[1].z))}; \
        *(LAS v4f*)&lds[L_CB + 8 * lane + 4] = v4f{rb(1.f + lo16(cb_[2].x)), rb(1.f + lo16(cb_[2].z)), rb(1.f + lo16(cb_[3].x)), rb(1.f + lo16(cb_[3].z))}; \
        if (lane < 16) { lds[L_SM + lane] = lo16(ga_.x); lds[L_SM + 16 + lane] = bf2f(xr); } \
        if (lane == 0) *(LAS v2f*)&lds[L_SM + 32] = v2f{lo16(gb_.x), lo16(gb_.z)}; \
        __all(ok); })
      if (!GATHER(0)) {
        for (unsigned spins = 0; spins < SPIN_CAP; ++spins) { __builtin_amdgcn_s_sleep(64); if (GATHER(AUX_SC1)) break; }
      }
    }
    if (w == 4) {
      LDSWAIT(5, 4u);
      const float mw = lds[L_ML + (lane & 3)];
      float M = fmaxf(mw, dpp<XOR1>(mw)); M = fmaxf(M, dpp<XOR2>(M));
      const int nq = (svc ? (split ? SZ_S4 : SZ_S0) : SZ_O) * 8;
      float L = 0.f;
      for (int i = lane; i < nq; i += 64) {
        const v4f s4 = *(const LAS v4f*)&lds[L_SC + 4 * i];
        L += (ex2((s4[0] - M) * LOG2E) + ex2((s4[1] - M) * LOG2E)) + (ex2((s4[2] - M) * LOG2E) + ex2((s4[3] - M) * LOG2E));
      }
      L = wave_sum(L);
      const v4u g2 = {tgf_lo | f24(M), tgf_hi, tgf_lo | f24(L), tgf_hi};
      if (lane == 0) __builtin_amdgcn_raw_buffer_store_b128(g2, rws, WS_ML + head * 128 + split * 16, 0, AUX_SC1);
      v4u hm;
      for (unsigned spins = 0; spins < SPIN_CAP; ++spins) {
        MEMB();
        hm = __builtin_amdgcn_raw_buffer_load_b128(rws, WS_ML + head * 128 + cg * 16, 0, AUX_SC1);
        if (__all(TOKF(hm))) break;
        __builtin_amdgcn_s_sleep(16);
      }
      const float mm = F24(hm.x);
      float MH = fmaxf(mm, dpp<XOR1>(mm)); MH = fmaxf(MH, dpp<XOR2>(MH)); MH = fmaxf(MH, dpp<XOR7>(MH));
      float LH = ex2((mm - MH) * LOG2E) * F24(hm.z); LH += dpp<XOR1>(LH); LH += dpp<XOR2>(LH); LH += dpp<XOR7>(LH);
      if (lane < 4) lds[L_ML + 8 + lane] = ex2((mw - MH) * LOG2E) / LH;
      LDSSET(3);
    }
    STAMP(3);
  }
  if (w < 4) {
    CONSV(R, false, rws, 0);
    STAMP(8);
    const float u0 = sw16_sum(acc[0], acc[1]), u1 = sw16_sum(acc[2], acc[3]), u2 = sw16_sum(acc[4], acc[5]), u3 = sw16_sum(acc[6], acc[7]);
    *(LAS v2f*)&lds[L_O + w * 128 + 2 * lane] = v2f{sw32_sum(u0, u1), sw32_sum(u2, u3)};
  }
  wg_barrier();
  STAMP(11);
  if (w == 4) {
    const v2f o2 = (*(const LAS v2f*)&lds[L_O + 2 * lane] + *(const LAS v2f*)&lds[L_O + 128 + 2 * lane]) + (*(const LAS v2f*)&lds[L_O + 256 + 2 * lane] + *(const LAS v2f*)&lds[L_O + 384 + 2 * lane]);
    const v4u gr = {tgf_lo | f24(o2.x), tgf_hi, tgf_lo | f24(o2.y), tgf_hi};
    __builtin_amdgcn_raw_buffer_store_b128(gr, rws, WS_OG + head * 8192 + rg * 1024 + split * 128 + cg * 16, 0, AUX_SC1);
  }
  {
    v4u og = {0, 0, 0, 0};
    bool ok = false;
    for (unsigned spins = 0; spins < SPIN_CAP; ++spins) {
      MEMB();
      if (!ok) { og = __builtin_amdgcn_raw_buffer_load_b128(rws, WS_OG + head * 8192 + w * 1024 + lane * 16, 0, AUX_SC1); ok = TOKF(og); }
      if (__all(ok)) break;
    }
    STAMP(12);
    float oa = F24(og.x), ob = F24(og.z);
    oa += dpp<XOR8>(oa); ob += dpp<XOR8>(ob);
    const float u = sw16_sum(oa, ob), z = sw32_sum(u, u);
    const float o = rb(z);
    float y0 = 0.f, y1 = 0.f;
#pragma unroll
    for (int i = 0; i < 8; ++i) { y0 = fmaf(rdl(o, i), wf[2 * i], y0); y1 = fmaf(rdl(o, 16 + i), wf[2 * i + 1], y1); }
    lds[L_RED + w * 64 + lane] = y0 + y1;
  }
  wg_barrier();
  if (w == 0 && lane < 32) {
    const v2f s = SUM4(L_RED) + SUM4(L_RED + 256);
    const v4u gr = {tgf_lo | f24(s.x), tgf_hi, tgf_lo | f24(s.y), tgf_hi};
    __builtin_amdgcn_raw_buffer_store_b128(gr, rws, WS_YG + (split * 4 + rg) * 4096 + head * 128 + cg * 16, 0, AUX_SC1);
  }
  STAMP(13);
  if (w == 6 && b < 32) {
    const int cc = 2 * cg + (r & 1);
    const float xc = lds[L_SM + 16 + cc], gc = lds[L_SM + cc];
    v4u yg[4];
    bool ok = false;
    for (unsigned spins = 0; spins < SPIN_CAP; ++spins) {
      MEMB();
      if (!ok) {
#pragma unroll
        for (int i = 0; i < 4; ++i) yg[i] = __builtin_amdgcn_raw_buffer_load_b128(rws, WS_YG + b * 4096 + i * 1024 + lane * 16, 0, AUX_SC1);
        ok = TOKF(yg[0]) & TOKF(yg[1]) & TOKF(yg[2]) & TOKF(yg[3]);
      }
      if (__all(ok)) break;
    }
    STAMP(1);
    float s0 = (F24(yg[0].x) + F24(yg[1].x)) + (F24(yg[2].x) + F24(yg[3].x)), s1 = (F24(yg[0].z) + F24(yg[1].z)) + (F24(yg[2].z) + F24(yg[3].z));
    s0 += dpp<XOR8>(s0); s1 += dpp<XOR8>(s1);
    const float u = sw16_sum(s0, s1), z = sw32_sum(u, u);
    const unsigned xb = bfbits(xc + rb(gc * rb(z)));
    v4u msg;
#pragma unroll
    for (int i = 0; i < 4; ++i) {
      const unsigned pa = (unsigned)__builtin_amdgcn_readlane((int)xb, i) | ((unsigned)__builtin_amdgcn_readlane((int)xb, 16 + i) << 16);
      const unsigned pb = (unsigned)__builtin_amdgcn_readlane((int)xb, 4 + i) | ((unsigned)__builtin_amdgcn_readlane((int)xb, 20 + i) << 16);
      const unsigned pk = (lane & 1) ? pb : pa;
      msg[i] = pk == SENT ? 0x7FC07FC0u : pk;
    }
    if (lane < 2 * NREP2) __builtin_amdgcn_raw_buffer_store_b128(msg, rws, WS_MB2 + (lane >> 1) * 1024 + b * 32 + (lane & 1) * 16, 0, AUX_SC1);
    STAMP(2);
  }
  if (w == 7) {
    float sb[8], cb[8];
    { const v4f a0 = *(const LAS v4f*)&lds[L_SB + 8 * lane], a1 = *(const LAS v4f*)&lds[L_SB + 8 * lane + 4], c0 = *(const LAS v4f*)&lds[L_CB + 8 * lane], c1 = *(const LAS v4f*)&lds[L_CB + 8 * lane + 4];
#pragma unroll
      for (int i = 0; i < 4; ++i) { sb[i] = a0[i]; sb[4 + i] = a1[i]; cb[i] = c0[i]; cb[4 + i] = c1[i]; } }
    v4u xs = {SENT, SENT, SENT, SENT};
    bool ok = false;
    for (unsigned spins = 0; spins < SPIN_CAP; ++spins) {
      MEMB();
      if (!ok) { xs = __builtin_amdgcn_raw_buffer_load_b128(rws, WS_MB2 + (b % NREP2) * 1024 + lane * 16, 0, AUX_SC1); ok = NSENT(xs); }
      if (__all(ok)) break;
    }
    STAMP(14);
    float xv[8];
#pragma unroll
    for (int i = 0; i < 4; ++i) { xv[2 * i] = lo16(xs[i]); xv[2 * i + 1] = hi16(xs[i]); }
    ln8(xv);
#pragma unroll
    for (int i = 0; i < 8; ++i) xv[i] = rb(rb(xv[i] * cb[i]) + sb[i]);
    ln8(xv);
    const int e_ = b & 3;
    const unsigned xw = e_ == 0 ? xs.x : e_ == 1 ? xs.y : e_ == 2 ? xs.z : xs.w;
    const float ha = e_ == 0 ? xv[0] : e_ == 1 ? xv[2] : e_ == 2 ? xv[4] : xv[6], hb = e_ == 0 ? xv[1] : e_ == 1 ? xv[3] : e_ == 2 ? xv[5] : xv[7];
    if (lane == (b >> 2)) *(LAS v4f*)&lds[L_SM + 36] = v4f{ha, hb, lo16(xw), hi16(xw)};
  }
  const float gbo = lds[L_SM + 32 + (lane & 1)];
  wg_barrier();
  STAMP(15);
  const v4f hx = *(const LAS v4f*)&lds[L_SM + 36];
  {
    float s0 = 0.f, s1 = 0.f;
#pragma unroll
    for (int i = 0; i < 3; ++i) {
#pragma unroll
      for (int d = 0; d < 2; ++d) {
        const float u = rb(rb(hx[d] * f1[i]) + f2[i]);
        const float z = u * fmaf(0.044715f * u, u, 1.f) * (-2.f * 0.7978845608028654f * LOG2E);
        const float gl = rb(u * rcp(1.f + ex2(z)));
        if (d == 0) s0 = fmaf(gl, f3[i], s0); else s1 = fmaf(gl, f3[i], s1);
      }
    }
    s0 = row_sum(s0); s1 = row_sum(s1);
    const float u = sw16_sum(s0, s1), z = sw32_sum(u, u);
    if (lane == 0 || lane == 16) lds[L_FIN + (lane >> 4) * 8 + w] = z;
  }
  wg_barrier();
  if (t < 2) {
    const v4f s0 = *(const LAS v4f*)&lds[L_FIN + t * 8], s1 = *(const LAS v4f*)&lds[L_FIN + t * 8 + 4];
    const float s = ((s0[0] + s0[1]) + (s0[2] + s0[3])) + ((s1[0] + s1[1]) + (s1[2] + s1[3]));
    const float yv = rb(rb(s) + b2f);
    p.out[2 * b + t] = (us)bfbits((t ? hx[3] : hx[2]) + rb(gbo * yv));
    if (b == 0 && t == 0) __hip_atomic_store((GAS u64*)(p.ws + WS_EPOCH), epoch + 1, RLX_AGENT);
  }
  STAMP(21);
}

extern "C" void kernel_launch(void* const* d_in, const int* in_sizes, int n_in, void* d_out, int out_size, void* d_ws, size_t ws_size, hipStream_t stream) {
  Params p;
  p.x = (const us*)d_in[0]; p.K = (const us*)d_in[1]; p.V = (const us*)d_in[2]; p.wq = (const us*)d_in[3]; p.wo = (const us*)d_in[4];
  p.w1 = (const us*)d_in[6]; p.b1 = (const us*)d_in[7]; p.w2 = (const us*)d_in[8]; p.b2 = (const us*)d_in[9];
  p.c = (const us*)d_in[10]; p.wmod = (const us*)d_in[11];
  p.out = (us*)d_out; p.ws = (unsigned char*)d_ws;
  fused_kernel<<<256, 512, 0, stream>>>(p);
}
```
